```python
import torch

def _softmax(x, axis=-1):
    return torch.softmax(x, dim=axis)

def _gelu(x):
    return torch.nn.functional.gelu(x, approximate='tanh')

def _silu(x):
    return torch.nn.functional.silu(x)

def _sigmoid(x):
    return torch.sigmoid(x)

def _layernorm(x, axis=-1, eps=1e-05):
    m = x.mean(dim=axis, keepdim=True)
    v = x.var(dim=axis, keepdim=True, unbiased=False)
    return (x - m) / torch.sqrt(v + eps)

def _scatter_add(tgt, idx, src, axis=0):
    return tgt.clone().index_add_(axis, idx, src)

def _segment_sum(src, seg, n):
    out = torch.zeros((n,) + src.shape[1:], dtype=src.dtype)
    return out.index_add_(0, seg, src)

def _pad(x, pw):
    flat = tuple((v for pair in reversed(pw) for v in pair))
    return torch.nn.functional.pad(x, flat)
kernel_name = 'adaLN_cond__gqa_block_cached__mixer_token_mlp__decode'

def setup_inputs():

    _w = lambda a: (lambda t: t.to(torch.bfloat16) if t.is_floating_point() else t)(torch.as_tensor(a))

    def _A_setup():
        B, Sq, D = (1, 1, 1024)
        Hq, Hkv, Sk, Dh = (32, 4, 65536, 128)
        assert Hq % Hkv == 0, (Hq, Hkv)
        return {'x': _w((lambda *s: torch.randn(*s, dtype=torch.bfloat16) * 0.1)(B, Sq, D)), 'k_cache': _w((lambda *s: torch.randn(*s, dtype=torch.bfloat16) * 3.2)(B, Hkv, Sk, Dh)), 'v_cache': _w((lambda *s: torch.randn(*s, dtype=torch.bfloat16) * 1.6)(B, Hkv, Sk, Dh)), 'wq': _w((lambda *s: torch.randn(*s, dtype=torch.bfloat16) * 3.2)(D, Hq * Dh) * D ** (-0.5)), 'wo': _w((lambda *s: torch.randn(*s, dtype=torch.bfloat16) * 1.6)(Hq * Dh, D) * (Hq * Dh) ** (-0.5))}
    _A_in = _A_setup()

    def _B_setup():
        B, S, D, F = (1, 1, 1024, 3584)
        return {'x': _w((lambda *s: torch.randn(*s, dtype=torch.bfloat16) * 0.1)(B, S, D)), 'w1': _w((lambda *s: torch.randn(*s, dtype=torch.bfloat16) * 1.6)(S, F) * S ** (-0.5)), 'b1': _w((lambda *s: torch.randn(*s, dtype=torch.bfloat16) * 1.6)(F) * 0.02), 'w2': _w((lambda *s: torch.randn(*s, dtype=torch.bfloat16) * 1.6)(F, S) * F ** (-0.5)), 'b2': _w((lambda *s: torch.randn(*s, dtype=torch.bfloat16) * 1.6)(S) * 0.02)}
    _B_in = _B_setup()
    return {'A_x': _A_in['x'], 'A_k_cache': _A_in['k_cache'], 'A_v_cache': _A_in['v_cache'], 'A_wq': _A_in['wq'], 'A_wo': _A_in['wo'], 'B_x': _B_in['x'], 'B_w1': _B_in['w1'], 'B_b1': _B_in['b1'], 'B_w2': _B_in['w2'], 'B_b2': _B_in['b2'], 'c': _w((lambda *s: torch.randn(*s, dtype=torch.bfloat16) * 0.1)(1, 1024) * 8.0), 'w_mod': _w((lambda *s: torch.randn(*s, dtype=torch.bfloat16) * 0.1)(1024, 6 * 1024) * 1024 ** (-0.5) * 8.0)}

def reference(A_x, A_k_cache, A_v_cache, A_wq, A_wo, B_x, B_w1, B_b1, B_w2, B_b2, c, w_mod):

    def _A_ref(x, k_cache, v_cache, wq, wo):
        B, Sq, D = x.shape
        Hq, Dh = (32, 128)
        Hkv = k_cache.shape[1]
        rep = Hq // Hkv
        q = (lambda x, *d: x.permute(*d))((x @ wq).reshape(B, Sq, Hq, Dh), 0, 2, 1, 3)
        k = (lambda x, n, axis: torch.repeat_interleave(x, n, dim=axis))(k_cache, rep, axis=1)
        v = (lambda x, n, axis: torch.repeat_interleave(x, n, dim=axis))(v_cache, rep, axis=1)
        scores = torch.einsum('bhqd,bhkd->bhqk', q, k) * Dh ** (-0.5)
        p = _softmax(scores, axis=-1)
        o = torch.einsum('bhqk,bhkd->bhqd', p, v)
        return (lambda x, *d: x.permute(*d))(o, 0, 2, 1, 3).reshape(B, Sq, Hq * Dh) @ wo

    def _B_ref(x, w1, b1, w2, b2):
        B, S, D = x.shape
        h = _layernorm(x, axis=-1)
        u = torch.einsum('bsd,sf->bdf', h, w1) + b1
        u = _gelu(u)
        y = torch.einsum('bdf,fs->bds', u, w2) + b2
        return (lambda x, *d: x.permute(*d))(y, 0, 2, 1)
    x = A_x
    mod = (_silu(c) @ w_mod).reshape(c.shape[0], 6, 1, -1)
    sa, ca, ga, sb, cb, gb = (mod[:, 0].clone(), mod[:, 1].clone(), mod[:, 2].clone(), mod[:, 3].clone(), mod[:, 4].clone(), mod[:, 5].clone())
    h = _layernorm(x, axis=-1) * (1 + ca) + sa
    x = x + ga * _A_ref(h, A_k_cache, A_v_cache, A_wq, A_wo)
    h = _layernorm(x, axis=-1) * (1 + cb) + sb
    x = x + gb * _B_ref(h, B_w1, B_b1, B_w2, B_b2)
    return x
```

```cpp
#include <hip/hip_runtime.h>
#include <hip/hip_bf16.h>
#include <stdint.h>

#define STAMP 0
#define LEAD 3
#define DLY1 44
#define DLY2 40
#define DLYA 14
#define DLYB 14
#define DLYC 14

namespace {
#define LAS __attribute__((address_space(3)))
typedef unsigned short u16;
typedef unsigned long long u64;
typedef unsigned v4u __attribute__((ext_vector_type(4)));
typedef unsigned v2u __attribute__((ext_vector_type(2)));
typedef float v4f __attribute__((ext_vector_type(4)));
typedef float v2f __attribute__((ext_vector_type(2)));
typedef short v4s __attribute__((ext_vector_type(4)));
typedef __bf16 v8bf __attribute__((ext_vector_type(8)));
typedef __bf16 v2bf __attribute__((ext_vector_type(2)));

constexpr int NCU = 256, NTHR = 512;
constexpr unsigned WS_E = 0, WS_GC = 4096, WS_GM = 16384, WS_G1 = 65536, WS_G3 = WS_G1 + 65536, WS_G2 = WS_G3 + 65536;
constexpr unsigned WS_GA = 1u << 20, WS_GB = 2u << 20, WS_ST = 3u << 20;
constexpr unsigned L_PRM = 131072, L_Q = L_PRM + 21504, L_P2 = L_Q + 2048, L_P3 = L_P2 + 512, L_P4 = L_P3 + 1024, L_H = L_P4 + 1024, L_ML = L_H + 1024, L_FAC = L_ML + 256,
                   L_O16 = L_FAC + 128, L_RB = L_O16 + 64, L_R1 = L_RB + 64, L_R2 = L_R1 + 32, L_H2 = L_R2 + 32, L_R3 = L_H2 + 16, L_TAG = L_R3 + 128,
                   L_CTL = L_TAG + 16, L_END = L_CTL + 64;
enum { C_P2 = 0, C_P3 = 1, C_Q = 2, C_P4 = 3, C_ML = 4, C_FAC = 5, C_LAST = 6 };
constexpr int SPIN_G = 1 << 17, SPIN_L = 1 << 22;
constexpr float LOG2E = 1.4426950408889634f, SCALE = 0.08838834764831845f;

struct Tag { unsigned a, b, c, d; };
struct Tag2 { unsigned a, b; };

__device__ __forceinline__ float asf(unsigned u) { return __builtin_bit_cast(float, u); }
__device__ __forceinline__ unsigned asu(float f) { return __builtin_bit_cast(unsigned, f); }
__device__ __forceinline__ unsigned pk2(float lo, float hi) { v2f p = {lo, hi}; return __builtin_bit_cast(unsigned, __builtin_convertvector(p, v2bf)); }
__device__ __forceinline__ float lo16(unsigned u) { return asf(u << 16); }
__device__ __forceinline__ float hi16(unsigned u) { return asf(u & 0xffff0000u); }
__device__ __forceinline__ float rbf(float f) { return lo16(pk2(f, f)); }
template <int C> __device__ __forceinline__ float dpp(float x) { return asf((unsigned)__builtin_amdgcn_mov_dpp((int)asu(x), C, 0xf, 0xf, true)); }
constexpr int XOR1 = 0xB1, XOR2 = 0x4E, HMIR = 0x141, ROR8 = 0x128, ROR4 = 0x124, ROR2 = 0x122, ROR1 = 0x121;
__device__ __forceinline__ float tr32(float a, float b) { auto r = __builtin_amdgcn_permlane32_swap(asu(a), asu(b), false, false); return asf(r[0]) + asf(r[1]); }
__device__ __forceinline__ float tr16(float a, float b) { auto r = __builtin_amdgcn_permlane16_swap(asu(a), asu(b), false, false); return asf(r[0]) + asf(r[1]); }
__device__ __forceinline__ float row_sum(float x) { x += dpp<ROR8>(x); x += dpp<ROR4>(x); x += dpp<ROR2>(x); x += dpp<ROR1>(x); return x; }
__device__ __forceinline__ float wave_sum(float x) { return row_sum(tr16(tr32(x, x), tr32(x, x))); }
__device__ __forceinline__ float wave_sum4(float a, float b, float c, float d) { return row_sum(tr16(tr32(a, c), tr32(b, d))); }
__device__ __forceinline__ float xrow_max(float x) {
  auto s = __builtin_amdgcn_permlane16_swap(asu(x), asu(x), false, false); x = fmaxf(asf(s[0]), asf(s[1]));
  auto t = __builtin_amdgcn_permlane32_swap(asu(x), asu(x), false, false); return fmaxf(asf(t[0]), asf(t[1]));
}
__device__ __forceinline__ float xrow_sum(float x) { x = tr16(x, x); return tr32(x, x); }

__device__ __forceinline__ unsigned tg(float v, unsigned t) { return ((asu(v) + 0x200u) & ~0x3ffu) | t; }
__device__ __forceinline__ float ug(unsigned v) { return asf(v & ~0x3ffu); }
__device__ __forceinline__ void g_store(const void* base, unsigned off, float a, float b, float c, float d, const Tag& t) {
  v4u v = {tg(a, t.a), tg(b, t.b), tg(c, t.c), tg(d, t.d)};
  asm volatile("global_store_dwordx4 %0, %1, %2 sc1\n\ts_nop 1" ::"v"(off), "v"(v), "s"(base) : "memory");
}
__device__ __forceinline__ bool g_ok(const v4u& x, const Tag& t) { return (((x.x ^ t.a) | (x.y ^ t.b) | (x.z ^ t.c) | (x.w ^ t.d)) & 0x3ffu) == 0u; }
__device__ __forceinline__ v4u g_wait1(const void* base, unsigned off, const Tag& t) {
  v4u x;
  for (int s = 0; s < SPIN_G; ++s) {
    asm volatile("global_load_dwordx4 %0, %1, %2 sc1\n\ts_waitcnt vmcnt(0)" : "=&v"(x) : "v"(off), "s"(base) : "memory");
    if (__all(g_ok(x, t))) break;
  }
  return x;
}
__device__ __forceinline__ void g_wait2(const void* base, unsigned o0, unsigned o1, const Tag& t, v4u& x0, v4u& x1, int nap) {
  for (int s = 0; s < SPIN_G; ++s) {
    asm volatile("global_load_dwordx4 %0, %2, %4 sc1\n\tglobal_load_dwordx4 %1, %3, %4 sc1\n\ts_waitcnt vmcnt(0)"
                 : "=&v"(x0), "=&v"(x1) : "v"(o0), "v"(o1), "s"(base) : "memory");
    if (__all(g_ok(x0, t) && g_ok(x1, t))) break;
    if (nap) __builtin_amdgcn_s_sleep(16);
  }
}
__device__ __forceinline__ void g_wait4(const void* base, unsigned o0, unsigned o1, unsigned o2, unsigned o3, const Tag& t, v4u& x0, v4u& x1, v4u& x2, v4u& x3) {
  for (int s = 0; s < SPIN_G; ++s) {
    asm volatile("global_load_dwordx4 %0, %4, %8 sc1\n\tglobal_load_dwordx4 %1, %5, %8 sc1\n\tglobal_load_dwordx4 %2, %6, %8 sc1\n\t"
                 "global_load_dwordx4 %3, %7, %8 sc1\n\ts_waitcnt vmcnt(0)"
                 : "=&v"(x0), "=&v"(x1), "=&v"(x2), "=&v"(x3) : "v"(o0), "v"(o1), "v"(o2), "v"(o3), "s"(base) : "memory");
    if (__all(g_ok(x0, t) && g_ok(x1, t) && g_ok(x2, t) && g_ok(x3, t))) break;
    __builtin_amdgcn_s_sleep(16);
  }
}

__device__ __forceinline__ void g2_store(const void* base, unsigned off, float a, float b, const Tag2& t) {
  v4u v = {asu(a), t.a, asu(b), t.b};
  asm volatile("global_store_dwordx4 %0, %1, %2 sc1\n\ts_nop 1" ::"v"(off), "v"(v), "s"(base) : "memory");
}
__device__ __forceinline__ bool g2_ok(const v4u& x, const Tag2& t) { return ((x.y ^ t.a) | (x.w ^ t.b)) == 0u; }
__device__ __forceinline__ void ld4(const void* base, unsigned o, unsigned st, v4u (&x)[4]) {
  asm volatile("global_load_dwordx4 %0, %4, %8 sc1\n\tglobal_load_dwordx4 %1, %5, %8 sc1\n\tglobal_load_dwordx4 %2, %6, %8 sc1\n\t"
               "global_load_dwordx4 %3, %7, %8 sc1\n\ts_waitcnt vmcnt(0)"
               : "=&v"(x[0]), "=&v"(x[1]), "=&v"(x[2]), "=&v"(x[3]) : "v"(o), "v"(o + st), "v"(o + 2 * st), "v"(o + 3 * st), "s"(base) : "memory");
}
__device__ __forceinline__ void ld8(const void* base, unsigned o, unsigned st, v4u (&x)[4], v4u (&y)[4]) {
  asm volatile("global_load_dwordx4 %0, %8, %12 sc1\n\tglobal_load_dwordx4 %1, %9, %12 sc1\n\tglobal_load_dwordx4 %2, %10, %12 sc1\n\t"
               "global_load_dwordx4 %3, %11, %12 sc1\n\tglobal_load_dwordx4 %4, %8, %13 sc1\n\tglobal_load_dwordx4 %5, %9, %13 sc1\n\t"
               "global_load_dwordx4 %6, %10, %13 sc1\n\tglobal_load_dwordx4 %7, %11, %13 sc1\n\ts_waitcnt vmcnt(0)"
               : "=&v"(x[0]), "=&v"(x[1]), "=&v"(x[2]), "=&v"(x[3]), "=&v"(y[0]), "=&v"(y[1]), "=&v"(y[2]), "=&v"(y[3])
               : "v"(o), "v"(o + st), "v"(o + 2 * st), "v"(o + 3 * st), "s"(base), "s"((const unsigned char*)base + 4096) : "memory");
}

#define LDSP(T, off) ((LAS T*)(L + (off)))
#define LGKM0() asm volatile("s_waitcnt lgkmcnt(0)" ::: "memory")
#define BAR() do { asm volatile("s_waitcnt lgkmcnt(0)" ::: "memory"); __builtin_amdgcn_s_barrier(); asm volatile("" ::: "memory"); } while (0)
__device__ __forceinline__ unsigned lds_arrive(LAS unsigned char* L, int c, int lane) {
  LGKM0();
  unsigned r = 0;
  if (lane == 0) r = __hip_atomic_fetch_add(LDSP(unsigned, L_CTL) + c, 1u, __ATOMIC_RELAXED, __HIP_MEMORY_SCOPE_WORKGROUP);
  r = __builtin_amdgcn_readfirstlane(r);
  asm volatile("" ::: "memory");
  return r;
}
__device__ __forceinline__ void lds_wait(LAS unsigned char* L, int c, unsigned n) {
  for (int s = 0; s < SPIN_L; ++s) {
    if (__builtin_amdgcn_readfirstlane(*(volatile LAS unsigned*)(LDSP(unsigned, L_CTL) + c)) >= n) break;
    __builtin_amdgcn_s_sleep(1);
  }
  asm volatile("" ::: "memory");
}
template <int N> __device__ __forceinline__ void wait_vm() { asm volatile("s_waitcnt vmcnt(%0)" ::"n"(N) : "memory"); }
__device__ __forceinline__ void dma_unit(unsigned lds_dst, const void* src, unsigned o0, unsigned o1, unsigned o2, unsigned o3) {
  unsigned keep;
  asm volatile("s_waitcnt lgkmcnt(0)\n\ts_mov_b32 %0, m0\n\ts_mov_b32 m0, %1\n\ts_nop 0\n\t"
               "global_load_lds_dwordx4 %3, %2 nt\n\tglobal_load_lds_dwordx4 %4, %2 offset:1024 nt\n\t"
               "global_load_lds_dwordx4 %5, %2 offset:2048 nt\n\tglobal_load_lds_dwordx4 %6, %2 offset:3072 nt\n\ts_mov_b32 m0, %0"
               : "=&s"(keep) : "s"(lds_dst), "s"(src), "v"(o0), "v"(o1), "v"(o2), "v"(o3) : "memory");
}
template <class T> __device__ __forceinline__ T ldnt(const void* p) { return __builtin_nontemporal_load((const T*)p); }
__device__ __forceinline__ void ln_stats(float s1, float s2, float& mb, float& rs) {
  const float mean = s1 * (1.f / 1024.f), var = fmaxf(s2 * (1.f / 1024.f) - mean * mean, 0.f);
  mb = rbf(mean);
  rs = __builtin_amdgcn_rcpf(rbf(__builtin_amdgcn_sqrtf(rbf(rbf(var) + 1e-5f))));
}
__device__ __forceinline__ void gemv64(const v4u (&w)[8], const float (&r)[8], int lane, LAS float* dst) {
  float acc[8] = {0, 0, 0, 0, 0, 0, 0, 0};
#pragma unroll
  for (int i = 0; i < 8; ++i)
#pragma unroll
    for (int j = 0; j < 4; ++j) { acc[2 * j] = fmaf(r[i], lo16(w[i][j]), acc[2 * j]); acc[2 * j + 1] = fmaf(r[i], hi16(w[i][j]), acc[2 * j + 1]); }
  const float c0 = tr16(tr32(acc[0], acc[4]), tr32(acc[2], acc[6])), c1 = tr16(tr32(acc[1], acc[5]), tr32(acc[3], acc[7]));
  const float d0 = c0 + dpp<ROR8>(c0), d1 = c1 + dpp<ROR8>(c1);
  dst[(lane & 7) * 8 + (lane >> 3)] = (lane & 8) ? d1 : d0;
}
constexpr unsigned L_TOTAL = L_END;
#define STS(i) do {} while (0)

__global__ void __launch_bounds__(NTHR, 2) block_kernel(const u16* x, const unsigned char* Kc, const unsigned char* Vc, const u16* wq, const u16* wo,
                                                         const u16* w1, const u16* b1, const u16* w2, const u16* b2, const u16* cvec, const u16* wmod,
                                                         u16* out, unsigned char* ws) {
  extern __shared__ __attribute__((aligned(16))) unsigned char smem[];
  LAS unsigned char* L = (LAS unsigned char*)smem;
  const int tid = threadIdx.x, lane = tid & 63, wave = __builtin_amdgcn_readfirstlane(tid >> 6);
  const int cu = blockIdx.x, g = cu >> 6, px = (cu & 7) * 32 + (cu >> 3);
  Tag tag;
  float x1own = 0.f, gbown = 0.f, b2v = 0.f;
  float T[4];
  STS(0);
  if (tid < 16) LDSP(unsigned, L_CTL)[tid] = 0;

  if (wave < 4) {
    const unsigned wb = wave * 32768u, lq = lane >> 4, lc = lane & 15;
    const unsigned char* kb = Kc + ((size_t)g * 65536 + (size_t)((cu & 63) * 4 + wave) * 16) * 256;
    const unsigned char* vb = Vc + ((size_t)g * 65536 + (size_t)((cu & 63) * 4 + wave) * 16) * 256;
    const unsigned ok0 = lq * 256 + ((lc ^ lq) << 4), ok1 = ok0 ^ 64, ok2 = ok0 ^ 128, ok3 = ok0 ^ 192;
    const unsigned ov0 = lq * 256 + ((lc ^ (lq << 1)) << 4), ov1 = ov0 ^ 128;
    unsigned ka[4], va[8];
#pragma unroll
    for (int s = 0; s < 4; ++s) ka[s] = wb + lc * 256 + (((4 * s + lq) ^ lc) << 4);
    { const unsigned row = 4 * lq + (lc >> 2);
#pragma unroll
      for (int dt = 0; dt < 8; ++dt) va[dt] = wb + row * 256 + ((dt ^ (row & 7)) << 5) + ((lane & 3) << 3); }
#define ISSUE(u) do { if ((u) < 16) dma_unit(wb + ((u) & 7) * 4096, kb + ((size_t)(u) << 20), ok0, ok1, ok2, ok3); \
                      else if ((u) < 32) dma_unit(wb + ((u) & 7) * 4096, vb + ((size_t)((u) - 16) << 20), ov0, ov1, ov0, ov1); } while (0)
#define LANDED(u) do { wait_vm<4 * (((u) + LEAD - 1 < 31 ? (u) + LEAD - 1 : 31) - (u))>(); STS(16 + (u)); } while (0)
    BAR();
#pragma unroll
    for (int u = 0; u < LEAD; ++u) ISSUE(u);
    v4u kr[8][4];
#define STEP_REG(u) do { LANDED(u); ISSUE((u) + LEAD); _Pragma("unroll") for (int s = 0; s < 4; ++s) kr[u][s] = *LDSP(v4u, ka[s] + (u) * 4096); } while (0)
    STEP_REG(0); STEP_REG(1); STEP_REG(2); STEP_REG(3); STEP_REG(4); STEP_REG(5); STEP_REG(6); STEP_REG(7);
#define STEP_PACE(u) do { if ((u) + LEAD < 16) { LANDED(u); ISSUE((u) + LEAD); } } while (0)
    STEP_PACE(8); STEP_PACE(9); STEP_PACE(10); STEP_PACE(11); STEP_PACE(12); STEP_PACE(13); STEP_PACE(14);
    STS(1);
    lds_wait(L, C_Q, 4);
    STS(2);
    v8bf qf[4];
#pragma unroll
    for (int s = 0; s < 4; ++s) qf[s] = __builtin_bit_cast(v8bf, *LDSP(v4u, L_Q + (lc & 7) * 256 + (4 * s + lq) * 16));
    { const v4u tv = *LDSP(v4u, L_TAG); tag = Tag{tv.x, tv.y, tv.z, tv.w}; }
    unsigned sc[16][2];
    float mx = -INFINITY;
    auto qk = [&](int u, const v4u (&kf)[4]) {
      v4f d = {0.f, 0.f, 0.f, 0.f};
#pragma unroll
      for (int s = 0; s < 4; ++s) d = __builtin_amdgcn_mfma_f32_16x16x32_bf16(__builtin_bit_cast(v8bf, kf[s]), qf[s], d, 0, 0, 0);
      mx = fmaxf(fmaxf(mx, fmaxf(d[0], d[1])), fmaxf(d[2], d[3]));
      const unsigned a = pk2(d[0], d[1]), b = pk2(d[2], d[3]);
      sc[u][0] = pk2(lo16(a) * SCALE, hi16(a) * SCALE); sc[u][1] = pk2(lo16(b) * SCALE, hi16(b) * SCALE);
    };
#define QK_LDS(u) do { v4u kf[4]; _Pragma("unroll") for (int s = 0; s < 4; ++s) kf[s] = *LDSP(v4u, ka[s] + ((u) & 7) * 4096); qk(u, kf); } while (0)
#define STEP_K(u) do { if ((u) + LEAD >= 16) { LANDED(u); ISSUE((u) + LEAD); } QK_LDS(u); } while (0)
#pragma unroll
    for (int u = 8; u < 16 - LEAD; ++u) QK_LDS(u);
#pragma unroll
    for (int u = 0; u < 8; ++u) qk(u, kr[u]);
    if (LEAD >= 7) STEP_K(9);
    if (LEAD >= 6) STEP_K(10);
    if (LEAD >= 5) STEP_K(11);
    if (LEAD >= 4) STEP_K(12);
    if (LEAD >= 3) STEP_K(13);
    if (LEAD >= 2) STEP_K(14);
    STEP_K(15);
    STS(3);
    {
      const float m = rbf(rbf(xrow_max(mx)) * SCALE), nm = -m * LOG2E;
      float l0 = 0.f, l1 = 0.f;
#pragma unroll
      for (int u = 0; u < 16; ++u)
#pragma unroll
        for (int j = 0; j < 2; ++j) {
          const float p0 = __builtin_amdgcn_exp2f(fmaf(lo16(sc[u][j]), LOG2E, nm)), p1 = __builtin_amdgcn_exp2f(fmaf(hi16(sc[u][j]), LOG2E, nm));
          l0 += p0; l1 += p1; sc[u][j] = pk2(p0, p1);
        }
      const float l = xrow_sum(l0 + l1);
      if (lane < 8) *LDSP(v2f, L_ML + wave * 64 + lane * 8) = v2f{m, l};
      lds_arrive(L, C_ML, lane);
    }
    STS(4);
    v4f acc[8];
#pragma unroll
    for (int dt = 0; dt < 8; ++dt) acc[dt] = v4f{0.f, 0.f, 0.f, 0.f};
#define STEP_V(k) do { v4s t1[8], t2[8]; \
      LANDED(16 + 2 * (k)); ISSUE(16 + 2 * (k) + LEAD); if (16 + 2 * (k) + LEAD == 31) lds_arrive(L, C_LAST, lane); \
      _Pragma("unroll") for (int dt = 0; dt < 8; ++dt) t1[dt] = __builtin_amdgcn_ds_read_tr16_b64_v4i16(LDSP(v4s, va[dt] + ((2 * (k)) & 7) * 4096)); \
      LANDED(17 + 2 * (k)); ISSUE(17 + 2 * (k) + LEAD); if (17 + 2 * (k) + LEAD == 31) lds_arrive(L, C_LAST, lane); \
      _Pragma("unroll") for (int dt = 0; dt < 8; ++dt) t2[dt] = __builtin_amdgcn_ds_read_tr16_b64_v4i16(LDSP(v4s, va[dt] + ((2 * (k) + 1) & 7) * 4096)); \
      const v8bf pf = __builtin_bit_cast(v8bf, v4u{sc[2 * (k)][0], sc[2 * (k)][1], sc[2 * (k) + 1][0], sc[2 * (k) + 1][1]}); \
      _Pragma("unroll") for (int dt = 0; dt < 8; ++dt) \
        acc[dt] = __builtin_amdgcn_mfma_f32_16x16x32_bf16(__builtin_bit_cast(v8bf, __builtin_shufflevector(t1[dt], t2[dt], 0, 1, 2, 3, 4, 5, 6, 7)), pf, acc[dt], 0, 0, 0); \
    } while (0)
    STEP_V(0); STEP_V(1); STEP_V(2); STEP_V(3); STEP_V(4); STEP_V(5); STEP_V(6);
    lds_wait(L, C_FAC, 1);
    const float fac = *LDSP(float, L_FAC + wave * 32 + (lc & 7) * 4);
    STS(5);
    STEP_V(7);
    STS(6);
    unsigned hm = (lc & 8) ? 0xffffffffu : 0u;
    asm volatile("" : "+v"(hm));
#pragma unroll
    for (int j = 0; j < 4; ++j) {
      v4f o;
#pragma unroll
      for (int r = 0; r < 4; ++r) o[r] = asf((asu(acc[j][r]) & ~hm) | (asu(acc[4 + j][r]) & hm)) * fac;
      *LDSP(v4f, wb + (lc & 7) * 528 + (j + ((lc >> 3) << 2)) * 64 + lq * 16) = o;
    }
    BAR();
    {
      const unsigned ro = (tid >> 5) * 528 + (tid & 31) * 16;
      const v4f o = (*LDSP(v4f, ro) + *LDSP(v4f, 32768 + ro)) + (*LDSP(v4f, 65536 + ro) + *LDSP(v4f, 98304 + ro));
      g_store(ws + WS_GA, (cu * 256 + tid) * 16, o[0], o[1], o[2], o[3], tag);
    }
    STS(7);
    __builtin_amdgcn_s_sleep(DLYA);
    {
      const v4u a = g_wait1(ws + WS_GA, (tid * 256 + px) * 16, tag);
      const float o = wave_sum4(ug(a.x), ug(a.y), ug(a.z), ug(a.w));
      if (lc == 0) *LDSP(u16, L_O16 + wave * 8 + lq * 2) = (u16)pk2(o, o);
    }
    STS(8);
    BAR();
    BAR();
    BAR();
  } else {
    __builtin_amdgcn_s_setprio(2);
    const int aw = wave - 4, ta = tid - 256;
    u64 e0;
    asm volatile("global_load_dwordx2 %0, %1, %2 sc1" : "=&v"(e0) : "v"(0u), "s"(ws + WS_E) : "memory");
    const int rb = px >> 6, cb = px & 63;
    const int R0 = 256 * rb + 64 * aw, row8 = R0 + 8 * (lane >> 3);
    const v4u cr = *(const v4u*)(cvec + row8);
    v4u wm[4], wqr[8];
#pragma unroll
    for (int k = 0; k < 4; ++k) wm[k] = ldnt<v4u>(wmod + (size_t)(R0 + 4 * (lane >> 2) + k) * 6144 + 32 * cb + 8 * (lane & 3));
    const v4u xa = *(const v4u*)(x + 16 * lane), xb = *(const v4u*)(x + 16 * lane + 8);
    const unsigned xr = *(const unsigned*)(x + R0 + 2 * (lane & 31));
#pragma unroll
    for (int i = 0; i < 8; ++i) wqr[i] = ldnt<v4u>(wq + (size_t)(row8 + i) * 4096 + 64 * cb + 8 * (lane & 7));
    BAR();
    asm volatile("s_waitcnt vmcnt(16)" : "+v"(e0)::"memory");
    const u64 e1 = e0 * 6364136223846793005ull + 1442695040888963407ull;
    tag = Tag{(unsigned)(e1 >> 24) & 0x3ffu, (unsigned)(e1 >> 34) & 0x3ffu, (unsigned)(e1 >> 44) & 0x3ffu, (unsigned)(e1 >> 54) & 0x3ffu};
    const Tag2 tag2{(unsigned)(e1 >> 32), (unsigned)e1};
    float sv[8];
#pragma unroll
    for (int k = 0; k < 8; ++k) { const float c = (k & 1) ? hi16(cr[k >> 1]) : lo16(cr[k >> 1]); sv[k] = rbf(c * __builtin_amdgcn_rcpf(1.f + __builtin_amdgcn_exp2f(-LOG2E * c))); }
    {
      unsigned hm = (lane & 4) ? 0xffffffffu : 0u;
      asm volatile("" : "+v"(hm));
      float a[8] = {0, 0, 0, 0, 0, 0, 0, 0};
#pragma unroll
      for (int k = 0; k < 4; ++k) {
        const float s = asf((asu(sv[k]) & ~hm) | (asu(sv[4 + k]) & hm));
#pragma unroll
        for (int j = 0; j < 4; ++j) { a[2 * j] = fmaf(s, lo16(wm[k][j]), a[2 * j]); a[2 * j + 1] = fmaf(s, hi16(wm[k][j]), a[2 * j + 1]); }
      }
      float c0 = tr16(tr32(a[0], a[4]), tr32(a[2], a[6])), c1 = tr16(tr32(a[1], a[5]), tr32(a[3], a[7]));
      c0 += dpp<ROR8>(c0); c1 += dpp<ROR8>(c1);
      const float d0 = c0 + dpp<ROR4>(c0), d1 = c1 + dpp<ROR4>(c1);
      if (!(lane & 8)) *LDSP(float, L_P2 + aw * 128 + ((lane & 3) * 8 + (lane >> 4) * 2 + ((lane >> 2) & 1)) * 4) = (lane & 4) ? d1 : d0;
      STS(1);
      if (lds_arrive(L, C_P2, lane) == 3 && lane < 16) {
        const v2f s = (*LDSP(v2f, L_P2 + lane * 8) + *LDSP(v2f, L_P2 + 128 + lane * 8)) + (*LDSP(v2f, L_P2 + 256 + lane * 8) + *LDSP(v2f, L_P2 + 384 + lane * 8));
        g2_store(ws + WS_G1, (rb * 1024 + 16 * cb + lane) * 16, s[0], s[1], tag2);
      }
    }
    float h0, h1;
    {
      float s1 = 0.f;
#pragma unroll
      for (int j = 0; j < 4; ++j) s1 += (lo16(xa[j]) + hi16(xa[j])) + (lo16(xb[j]) + hi16(xb[j]));
      s1 = wave_sum(s1);
      const float mean = s1 * (1.f / 1024.f);
      float s2 = 0.f;
#pragma unroll
      for (int j = 0; j < 4; ++j) { float d = lo16(xa[j]) - mean; s2 = fmaf(d, d, s2); d = hi16(xa[j]) - mean; s2 = fmaf(d, d, s2);
                                    d = lo16(xb[j]) - mean; s2 = fmaf(d, d, s2); d = hi16(xb[j]) - mean; s2 = fmaf(d, d, s2); }
      s2 = wave_sum(s2);
      const float mb = rbf(mean), rs = __builtin_amdgcn_rcpf(rbf(__builtin_amdgcn_sqrtf(rbf(rbf(s2 * (1.f / 1024.f)) + 1e-5f))));
      h0 = rbf(rbf(lo16(xr) - mb) * rs); h1 = rbf(rbf(hi16(xr) - mb) * rs);
    }
    __builtin_amdgcn_s_sleep(DLY1);
    {
      v4u p[4];
      const unsigned o = ((lane >> 5) * 512 + R0 / 2 + (lane & 31)) * 16;
      for (int s = 0; s < SPIN_G; ++s) { ld4(ws + WS_G1, o, 16384, p); if (__all(g2_ok(p[0], tag2) && g2_ok(p[1], tag2) && g2_ok(p[2], tag2) && g2_ok(p[3], tag2))) break; }
      const float m0 = rbf((asf(p[0].x) + asf(p[1].x)) + (asf(p[2].x) + asf(p[3].x))), m1 = rbf((asf(p[0].z) + asf(p[1].z)) + (asf(p[2].z) + asf(p[3].z)));
      const auto r0 = __builtin_amdgcn_permlane32_swap(asu(m0), asu(m0), false, false), r1 = __builtin_amdgcn_permlane32_swap(asu(m1), asu(m1), false, false);
      h0 = rbf(rbf(h0 * rbf(1.f + asf(r0[1]))) + asf(r0[0])); h1 = rbf(rbf(h1 * rbf(1.f + asf(r1[1]))) + asf(r1[0]));
      if (lane < 32) *LDSP(v2f, L_H + aw * 256 + lane * 8) = v2f{h0, h1};
    }
    STS(2);
    {
      const v4f ha = *LDSP(v4f, L_H + aw * 256 + (lane >> 3) * 32), hb = *LDSP(v4f, L_H + aw * 256 + (lane >> 3) * 32 + 16);
      const float hv[8] = {ha[0], ha[1], ha[2], ha[3], hb[0], hb[1], hb[2], hb[3]};
      gemv64(wqr, hv, lane, LDSP(float, L_P3 + aw * 256));
    }
    if (lds_arrive(L, C_P3, lane) == 3) {
      if (lane < 32) {
        const v2f s = (*LDSP(v2f, L_P3 + lane * 8) + *LDSP(v2f, L_P3 + 256 + lane * 8)) + (*LDSP(v2f, L_P3 + 512 + lane * 8) + *LDSP(v2f, L_P3 + 768 + lane * 8));
        g2_store(ws + WS_G2, (rb * 2048 + 32 * cb + lane) * 16, s[0], s[1], tag2);
      }
    }
    STS(3);
    __builtin_amdgcn_s_sleep(DLY2);
    {
      v4u p[4], q[4];
      const unsigned o = (g * 512 + ta) * 16;
      for (int s = 0; s < SPIN_G; ++s) {
        ld8(ws + WS_G2, o, 32768, p, q);
        if (__all(g2_ok(p[0], tag2) && g2_ok(p[1], tag2) && g2_ok(p[2], tag2) && g2_ok(p[3], tag2) && g2_ok(q[0], tag2) && g2_ok(q[1], tag2) && g2_ok(q[2], tag2) && g2_ok(q[3], tag2))) break;
      }
      *LDSP(unsigned, L_Q + ta * 4) = pk2((asf(p[0].x) + asf(p[1].x)) + (asf(p[2].x) + asf(p[3].x)), (asf(p[0].z) + asf(p[1].z)) + (asf(p[2].z) + asf(p[3].z)));
      *LDSP(unsigned, L_Q + 1024 + ta * 4) = pk2((asf(q[0].x) + asf(q[1].x)) + (asf(q[2].x) + asf(q[3].x)), (asf(q[0].z) + asf(q[1].z)) + (asf(q[2].z) + asf(q[3].z)));
      if (ta == 0) *LDSP(v4u, L_TAG) = v4u{tag.a, tag.b, tag.c, tag.d};
      lds_arrive(L, C_Q, lane);
    }
    STS(4);
    {
      v4u wr[8];
#pragma unroll
      for (int i = 0; i < 8; ++i) wr[i] = ldnt<v4u>(wmod + (size_t)(row8 + i) * 6144 + 2048 + 64 * cb + 8 * (lane & 7));
      v4u pr[6];
#pragma unroll
      for (int k = 0; k < 2; ++k) { const int ch = min(ta + 256 * k, 447);
        pr[k] = *(const v4u*)(w1 + 8 * ch); pr[2 + k] = *(const v4u*)(b1 + 8 * ch); pr[4 + k] = *(const v4u*)(w2 + 8 * ch); }
      gemv64(wr, sv, lane, LDSP(float, L_P4 + aw * 256));
      if (lds_arrive(L, C_P4, lane) == 3 && lane < 16) {
        const v4f s = (*LDSP(v4f, L_P4 + lane * 16) + *LDSP(v4f, L_P4 + 256 + lane * 16)) + (*LDSP(v4f, L_P4 + 512 + lane * 16) + *LDSP(v4f, L_P4 + 768 + lane * 16));
        g_store(ws + WS_G3, (rb * 1024 + 16 * cb + lane) * 16, s[0], s[1], s[2], s[3], tag);
      }
#pragma unroll
      for (int k = 0; k < 2; ++k) { const int ch = min(ta + 256 * k, 447);
        *LDSP(v4u, L_PRM + ch * 16) = pr[k]; *LDSP(v4u, L_PRM + 7168 + ch * 16) = pr[2 + k]; *LDSP(v4u, L_PRM + 14336 + ch * 16) = pr[4 + k]; }
    }
    STS(5);
    lds_wait(L, C_ML, 4);
    STS(6);
    if (aw == 0) {
      const int h = 2 * (lane & 3);
      float mw[4][2], lw[4][2];
#pragma unroll
      for (int w = 0; w < 4; ++w) { const v4f v = *LDSP(v4f, L_ML + w * 64 + h * 8); mw[w][0] = v[0]; lw[w][0] = v[1]; mw[w][1] = v[2]; lw[w][1] = v[3]; }
      float M[2], S[2];
#pragma unroll
      for (int j = 0; j < 2; ++j) {
        M[j] = fmaxf(fmaxf(mw[0][j], mw[1][j]), fmaxf(mw[2][j], mw[3][j])); S[j] = 0.f;
#pragma unroll
        for (int w = 0; w < 4; ++w) S[j] = fmaf(lw[w][j], __builtin_amdgcn_exp2f((mw[w][j] - M[j]) * LOG2E), S[j]);
      }
      if (lane < 4) g_store(ws + WS_GM, (cu * 4 + lane) * 16, M[0], S[0], M[1], S[1], tag);
      __builtin_amdgcn_s_sleep(100);
      v4u r[4];
      const unsigned o = (g * 256 + lane) * 16;
      g_wait4(ws + WS_GM, o, o + 1024, o + 2048, o + 3072, tag, r[0], r[1], r[2], r[3]);
#pragma unroll
      for (int j = 0; j < 2; ++j) {
        float m = fmaxf(fmaxf(ug(r[0][2 * j]), ug(r[1][2 * j])), fmaxf(ug(r[2][2 * j]), ug(r[3][2 * j])));
        m = fmaxf(m, dpp<ROR8>(m)); m = fmaxf(m, dpp<ROR4>(m)); m = xrow_max(m);
        float s = 0.f;
#pragma unroll
        for (int k = 0; k < 4; ++k) s = fmaf(ug(r[k][2 * j + 1]), __builtin_amdgcn_exp2f((ug(r[k][2 * j]) - m) * LOG2E), s);
        s += dpp<ROR8>(s); s += dpp<ROR4>(s); s = xrow_sum(s);
        const float inv = 1.f / s;
        if (lane < 4) {
#pragma unroll
          for (int w = 0; w < 4; ++w) *LDSP(float, L_FAC + w * 32 + (h + j) * 4) = __builtin_amdgcn_exp2f((mw[w][j] - m) * LOG2E) * inv;
        }
      }
      lds_arrive(L, C_FAC, lane);
    }
    float c1[4], sb[4], gaown;
    {
      v4u p[4], q[4], u[4], v[4];
      for (int s = 0; s < SPIN_G; ++s) {
        ld8(ws + WS_G3, (256 + ta) * 16, 16384, p, q);
        if (__all(g_ok(p[0], tag) && g_ok(p[1], tag) && g_ok(p[2], tag) && g_ok(p[3], tag) && g_ok(q[0], tag) && g_ok(q[1], tag) && g_ok(q[2], tag) && g_ok(q[3], tag))) break;
        __builtin_amdgcn_s_sleep(16);
      }
#pragma unroll
      for (int k = 0; k < 4; ++k) {
        sb[k] = rbf((ug(p[0][k]) + ug(p[1][k])) + (ug(p[2][k]) + ug(p[3][k])));
        c1[k] = rbf(1.f + rbf((ug(q[0][k]) + ug(q[1][k])) + (ug(q[2][k]) + ug(q[3][k]))));
      }
      g_wait4(ws + WS_G3, px * 16, 16384 + px * 16, 32768 + px * 16, 49152 + px * 16, tag, u[0], u[1], u[2], u[3]);
      g_wait4(ws + WS_G3, 12288 + px * 16, 28672 + px * 16, 45056 + px * 16, 61440 + px * 16, tag, v[0], v[1], v[2], v[3]);
      const int c = lane & 3;
      float ga4[4], gb4[4];
#pragma unroll
      for (int k = 0; k < 4; ++k) { ga4[k] = (ug(u[0][k]) + ug(u[1][k])) + (ug(u[2][k]) + ug(u[3][k])); gb4[k] = (ug(v[0][k]) + ug(v[1][k])) + (ug(v[2][k]) + ug(v[3][k])); }
      gaown = rbf(c == 0 ? ga4[0] : c == 1 ? ga4[1] : c == 2 ? ga4[2] : ga4[3]);
      gbown = rbf(c == 0 ? gb4[0] : c == 1 ? gb4[1] : c == 2 ? gb4[2] : gb4[3]);
      x1own = lo16(x[4 * px + c]);
      b2v = lo16(b2[0]);
    }
    STS(7);
    lds_wait(L, C_LAST, 4);
    v2u wor[16];
#pragma unroll
    for (int r = 0; r < 16; ++r) wor[r] = ldnt<v2u>(wo + (size_t)((r >> 2) * 1024 + 4 * px + (r & 3)) * 1024 + 4 * ta);
    BAR();
    unsigned wp[8][4];
#pragma unroll
    for (int p = 0; p < 8; ++p) {
      asm volatile("" : "+v"(wor[2 * p]), "+v"(wor[2 * p + 1]));
      wp[p][0] = __builtin_amdgcn_perm(wor[2 * p + 1].x, wor[2 * p].x, 0x05040100u); wp[p][1] = __builtin_amdgcn_perm(wor[2 * p + 1].x, wor[2 * p].x, 0x07060302u);
      wp[p][2] = __builtin_amdgcn_perm(wor[2 * p + 1].y, wor[2 * p].y, 0x05040100u); wp[p][3] = __builtin_amdgcn_perm(wor[2 * p + 1].y, wor[2 * p].y, 0x07060302u);
      asm volatile("" : "+v"(wp[p][0]), "+v"(wp[p][1]), "+v"(wp[p][2]), "+v"(wp[p][3]));
    }
    STS(8);
    BAR();
    {
      const v4u oa = *LDSP(v4u, L_O16), ob = *LDSP(v4u, L_O16 + 16);
      float a[4] = {0.f, 0.f, 0.f, 0.f};
#pragma unroll
      for (int p = 0; p < 8; ++p)
#pragma unroll
        for (int c = 0; c < 4; ++c)
          a[c] = __builtin_amdgcn_fdot2_f32_bf16(__builtin_bit_cast(v2bf, wp[p][c]), __builtin_bit_cast(v2bf, p < 4 ? oa[p] : ob[p - 4]), a[c], false);
      g_store(ws + WS_GB, (cu * 256 + ta) * 16, a[0], a[1], a[2], a[3], tag);
    }
    STS(9);
    __builtin_amdgcn_s_sleep(DLYB);
    {
      const v4u a = g_wait1(ws + WS_GB, (ta * 256 + px) * 16, tag);
      const float s = wave_sum4(ug(a.x), ug(a.y), ug(a.z), ug(a.w));
      if ((lane & 15) == 0) *LDSP(float, L_RB + (lane >> 4) * 16 + aw * 4) = s;
    }
    STS(10);
    BAR();
    if (aw == 0) {
      const v4f s = *LDSP(v4f, L_RB + (lane & 3) * 16);
      x1own = rbf(x1own + rbf(gaown * rbf((s[0] + s[1]) + (s[2] + s[3]))));
      const float a0 = dpp<0x00>(x1own), a1 = dpp<0x55>(x1own), a2 = dpp<0xAA>(x1own), a3 = dpp<0xFF>(x1own);
      if (lane == 0) g_store(ws + WS_GC, px * 16, a0, a1, a2, a3, tag);
    }
    STS(11);
    __builtin_amdgcn_s_sleep(DLYC);
    float h2[4];
    {
      const v4u a = g_wait1(ws + WS_GC, ta * 16, tag);
      if (cu == 0 && ta == 0) asm volatile("global_store_dwordx2 %0, %1, %2 sc1" ::"v"(0u), "v"(e1), "s"(ws + WS_E) : "memory");
      STS(12);
#pragma unroll
      for (int k = 0; k < 4; ++k) h2[k] = ug(a[k]);
      const float u = tr32((h2[0] + h2[1]) + (h2[2] + h2[3]), fmaf(h2[0], h2[0], h2[1] * h2[1]) + fmaf(h2[2], h2[2], h2[3] * h2[3]));
      const float s = row_sum(tr16(u, u));
      if ((lane & 31) == 0) *LDSP(float, L_R1 + (lane >> 5) * 16 + aw * 4) = s;
    }
    BAR();
    {
      const v4f p = *LDSP(v4f, L_R1), q = *LDSP(v4f, L_R1 + 16);
      float mb, rs;
      ln_stats((p[0] + p[1]) + (p[2] + p[3]), (q[0] + q[1]) + (q[2] + q[3]), mb, rs);
#pragma unroll
      for (int k = 0; k < 4; ++k) h2[k] = rbf(rbf(rbf(rbf(h2[k] - mb) * rs) * c1[k]) + sb[k]);
      const float u = tr32((h2[0] + h2[1]) + (h2[2] + h2[3]), fmaf(h2[0], h2[0], h2[1] * h2[1]) + fmaf(h2[2], h2[2], h2[3] * h2[3]));
      const float s = row_sum(tr16(u, u));
      if ((lane & 31) == 0) *LDSP(float, L_R2 + (lane >> 5) * 16 + aw * 4) = s;
      if (ta == px) *LDSP(v4f, L_H2) = v4f{h2[0], h2[1], h2[2], h2[3]};
    }
  }
  float pw1[7], pb1[7], pw2[7];
#pragma unroll
  for (int k = 0; k < 7; ++k) {
    pw1[k] = lo16(*LDSP(u16, L_PRM + (tid + 512 * k) * 2)); pb1[k] = lo16(*LDSP(u16, L_PRM + 7168 + (tid + 512 * k) * 2));
    pw2[k] = lo16(*LDSP(u16, L_PRM + 14336 + (tid + 512 * k) * 2));
  }
  BAR();
  {
    const v4f p = *LDSP(v4f, L_R2), q = *LDSP(v4f, L_R2 + 16), h = *LDSP(v4f, L_H2);
    float mb, rs;
    ln_stats((p[0] + p[1]) + (p[2] + p[3]), (q[0] + q[1]) + (q[2] + q[3]), mb, rs);
#pragma unroll
    for (int k = 0; k < 4; ++k) T[k] = rbf(rbf(h[k] - mb) * rs);
  }
  STS(13);
  {
    float y[4] = {0.f, 0.f, 0.f, 0.f};
    constexpr float G0 = -2.f * 0.7978845608028654f * LOG2E, G1 = G0 * 0.044715f;
#pragma unroll
    for (int k = 0; k < 7; ++k)
#pragma unroll
      for (int c = 0; c < 4; c += 2) {
        const unsigned a = pk2(T[c] * pw1[k], T[c + 1] * pw1[k]);
        const unsigned u = pk2(lo16(a) + pb1[k], hi16(a) + pb1[k]);
        const float u0 = lo16(u), u1 = hi16(u);
        const float e0 = __builtin_amdgcn_exp2f(u0 * fmaf(u0 * u0, G1, G0)), e1 = __builtin_amdgcn_exp2f(u1 * fmaf(u1 * u1, G1, G0));
        const unsigned gl = pk2(u0 * __builtin_amdgcn_rcpf(1.f + e0), u1 * __builtin_amdgcn_rcpf(1.f + e1));
        y[c] = fmaf(lo16(gl), pw2[k], y[c]); y[c + 1] = fmaf(hi16(gl), pw2[k], y[c + 1]);
      }
    const float s = wave_sum4(y[0], y[1], y[2], y[3]);
    if ((lane & 15) == 0) *LDSP(float, L_R3 + (lane >> 4) * 32 + wave * 4) = s;
  }
  STS(14);
  BAR();
  if (wave == 4 && lane < 4) {
    const v4f p = *LDSP(v4f, L_R3 + lane * 32), q = *LDSP(v4f, L_R3 + lane * 32 + 16);
    const float y = rbf(rbf(((p[0] + p[1]) + (p[2] + p[3])) + ((q[0] + q[1]) + (q[2] + q[3]))) + b2v);
    const float o = x1own + rbf(gbown * y);
    out[4 * px + lane] = (u16)pk2(o, o);
  }
}
}

extern "C" void kernel_launch(void* const* d_in, const int* in_sizes, int n_in, void* d_out, int out_size, void* d_ws, size_t ws_size, hipStream_t stream) {
  (void)in_sizes; (void)n_in; (void)out_size; (void)ws_size;
  (void)hipFuncSetAttribute((const void*)block_kernel, hipFuncAttributeMaxDynamicSharedMemorySize, L_TOTAL);
  hipLaunchKernelGGL(block_kernel, dim3(NCU), dim3(NTHR), L_TOTAL, stream, (const u16*)d_in[0], (const unsigned char*)d_in[1], (const unsigned char*)d_in[2],
                     (const u16*)d_in[3], (const u16*)d_in[4], (const u16*)d_in[6], (const u16*)d_in[7], (const u16*)d_in[8], (const u16*)d_in[9],
                     (const u16*)d_in[10], (const u16*)d_in[11], (u16*)d_out, (unsigned char*)d_ws);
}
```

```cpp
#include <hip/hip_runtime.h>
#include <hip/hip_bf16.h>
#include <stdint.h>

#define STAMP 1
#define LEAD 3
#define DLY1 44
#define DLY2 40
#define DLYA 14
#define DLYB 14
#define DLYC 14

namespace {
#define LAS __attribute__((address_space(3)))
typedef unsigned short u16;
typedef unsigned long long u64;
typedef unsigned v4u __attribute__((ext_vector_type(4)));
typedef unsigned v2u __attribute__((ext_vector_type(2)));
typedef float v4f __attribute__((ext_vector_type(4)));
typedef float v2f __attribute__((ext_vector_type(2)));
typedef short v4s __attribute__((ext_vector_type(4)));
typedef __bf16 v8bf __attribute__((ext_vector_type(8)));
typedef __bf16 v2bf __attribute__((ext_vector_type(2)));

constexpr int NCU = 256, NTHR = 512;
constexpr unsigned WS_E = 0, WS_GC = 4096, WS_GM = 16384, WS_G1 = 65536, WS_G3 = WS_G1 + 65536, WS_G2 = WS_G3 + 65536;
constexpr unsigned WS_GA = 1u << 20, WS_GB = 2u << 20, WS_ST = 3u << 20;
constexpr unsigned L_PRM = 131072, L_Q = L_PRM + 21504, L_P2 = L_Q + 2048, L_P3 = L_P2 + 512, L_P4 = L_P3 + 1024, L_H = L_P4 + 1024, L_ML = L_H + 1024, L_FAC = L_ML + 256,
                   L_O16 = L_FAC + 128, L_RB = L_O16 + 64, L_R1 = L_RB + 64, L_R2 = L_R1 + 32, L_H2 = L_R2 + 32, L_R3 = L_H2 + 16, L_TAG = L_R3 + 128,
                   L_CTL = L_TAG + 16, L_END = L_CTL + 64;
enum { C_P2 = 0, C_P3 = 1, C_Q = 2, C_P4 = 3, C_ML = 4, C_FAC = 5, C_LAST = 6 };
constexpr int SPIN_G = 1 << 17, SPIN_L = 1 << 22;
constexpr float LOG2E = 1.4426950408889634f, SCALE = 0.08838834764831845f;

struct Tag { unsigned a, b, c, d; };
struct Tag2 { unsigned a, b; };

__device__ __forceinline__ float asf(unsigned u) { return __builtin_bit_cast(float, u); }
__device__ __forceinline__ unsigned asu(float f) { return __builtin_bit_cast(unsigned, f); }
__device__ __forceinline__ unsigned pk2(float lo, float hi) { v2f p = {lo, hi}; return __builtin_bit_cast(unsigned, __builtin_convertvector(p, v2bf)); }
__device__ __forceinline__ float lo16(unsigned u) { return asf(u << 16); }
__device__ __forceinline__ float hi16(unsigned u) { return asf(u & 0xffff0000u); }
__device__ __forceinline__ float rbf(float f) { return lo16(pk2(f, f)); }
template <int C> __device__ __forceinline__ float dpp(float x) { return asf((unsigned)__builtin_amdgcn_mov_dpp((int)asu(x), C, 0xf, 0xf, true)); }
constexpr int XOR1 = 0xB1, XOR2 = 0x4E, HMIR = 0x141, ROR8 = 0x128, ROR4 = 0x124, ROR2 = 0x122, ROR1 = 0x121;
__device__ __forceinline__ float tr32(float a, float b) { auto r = __builtin_amdgcn_permlane32_swap(asu(a), asu(b), false, false); return asf(r[0]) + asf(r[1]); }
__device__ __forceinline__ float tr16(float a, float b) { auto r = __builtin_amdgcn_permlane16_swap(asu(a), asu(b), false, false); return asf(r[0]) + asf(r[1]); }
__device__ __forceinline__ float row_sum(float x) { x += dpp<ROR8>(x); x += dpp<ROR4>(x); x += dpp<ROR2>(x); x += dpp<ROR1>(x); return x; }
__device__ __forceinline__ float wave_sum(float x) { return row_sum(tr16(tr32(x, x), tr32(x, x))); }
__device__ __forceinline__ float wave_sum4(float a, float b, float c, float d) { return row_sum(tr16(tr32(a, c), tr32(b, d))); }
__device__ __forceinline__ float xrow_max(float x) {
  auto s = __builtin_amdgcn_permlane16_swap(asu(x), asu(x), false, false); x = fmaxf(asf(s[0]), asf(s[1]));
  auto t = __builtin_amdgcn_permlane32_swap(asu(x), asu(x), false, false); return fmaxf(asf(t[0]), asf(t[1]));
}
__device__ __forceinline__ float xrow_sum(float x) { x = tr16(x, x); return tr32(x, x); }

__device__ __forceinline__ unsigned tg(float v, unsigned t) { return ((asu(v) + 0x200u) & ~0x3ffu) | t; }
__device__ __forceinline__ float ug(unsigned v) { return asf(v & ~0x3ffu); }
__device__ __forceinline__ void g_store(const void* base, unsigned off, float a, float b, float c, float d, const Tag& t) {
  v4u v = {tg(a, t.a), tg(b, t.b), tg(c, t.c), tg(d, t.d)};
  asm volatile("global_store_dwordx4 %0, %1, %2 sc1\n\ts_nop 1" ::"v"(off), "v"(v), "s"(base) : "memory");
}
__device__ __forceinline__ bool g_ok(const v4u& x, const Tag& t) { return (((x.x ^ t.a) | (x.y ^ t.b) | (x.z ^ t.c) | (x.w ^ t.d)) & 0x3ffu) == 0u; }
__device__ __forceinline__ v4u g_wait1(const void* base, unsigned off, const Tag& t) {
  v4u x;
  for (int s = 0; s < SPIN_G; ++s) {
    asm volatile("global_load_dwordx4 %0, %1, %2 sc1\n\ts_waitcnt vmcnt(0)" : "=&v"(x) : "v"(off), "s"(base) : "memory");
    if (__all(g_ok(x, t))) break;
  }
  return x;
}
__device__ __forceinline__ void g_wait2(const void* base, unsigned o0, unsigned o1, const Tag& t, v4u& x0, v4u& x1, int nap) {
  for (int s = 0; s < SPIN_G; ++s) {
    asm volatile("global_load_dwordx4 %0, %2, %4 sc1\n\tglobal_load_dwordx4 %1, %3, %4 sc1\n\ts_waitcnt vmcnt(0)"
                 : "=&v"(x0), "=&v"(x1) : "v"(o0), "v"(o1), "s"(base) : "memory");
    if (__all(g_ok(x0, t) && g_ok(x1, t))) break;
    if (nap) __builtin_amdgcn_s_sleep(16);
  }
}
__device__ __forceinline__ void g_wait4(const void* base, unsigned o0, unsigned o1, unsigned o2, unsigned o3, const Tag& t, v4u& x0, v4u& x1, v4u& x2, v4u& x3) {
  for (int s = 0; s < SPIN_G; ++s) {
    asm volatile("global_load_dwordx4 %0, %4, %8 sc1\n\tglobal_load_dwordx4 %1, %5, %8 sc1\n\tglobal_load_dwordx4 %2, %6, %8 sc1\n\t"
                 "global_load_dwordx4 %3, %7, %8 sc1\n\ts_waitcnt vmcnt(0)"
                 : "=&v"(x0), "=&v"(x1), "=&v"(x2), "=&v"(x3) : "v"(o0), "v"(o1), "v"(o2), "v"(o3), "s"(base) : "memory");
    if (__all(g_ok(x0, t) && g_ok(x1, t) && g_ok(x2, t) && g_ok(x3, t))) break;
    __builtin_amdgcn_s_sleep(16);
  }
}

__device__ __forceinline__ void g2_store(const void* base, unsigned off, float a, float b, const Tag2& t) {
  v4u v = {asu(a), t.a, asu(b), t.b};
  asm volatile("global_store_dwordx4 %0, %1, %2 sc1\n\ts_nop 1" ::"v"(off), "v"(v), "s"(base) : "memory");
}
__device__ __forceinline__ bool g2_ok(const v4u& x, const Tag2& t) { return ((x.y ^ t.a) | (x.w ^ t.b)) == 0u; }
__device__ __forceinline__ void ld4(const void* base, unsigned o, unsigned st, v4u (&x)[4]) {
  asm volatile("global_load_dwordx4 %0, %4, %8 sc1\n\tglobal_load_dwordx4 %1, %5, %8 sc1\n\tglobal_load_dwordx4 %2, %6, %8 sc1\n\t"
               "global_load_dwordx4 %3, %7, %8 sc1\n\ts_waitcnt vmcnt(0)"
               : "=&v"(x[0]), "=&v"(x[1]), "=&v"(x[2]), "=&v"(x[3]) : "v"(o), "v"(o + st), "v"(o + 2 * st), "v"(o + 3 * st), "s"(base) : "memory");
}
__device__ __forceinline__ void ld8(const void* base, unsigned o, unsigned st, v4u (&x)[4], v4u (&y)[4]) {
  asm volatile("global_load_dwordx4 %0, %8, %12 sc1\n\tglobal_load_dwordx4 %1, %9, %12 sc1\n\tglobal_load_dwordx4 %2, %10, %12 sc1\n\t"
               "global_load_dwordx4 %3, %11, %12 sc1\n\tglobal_load_dwordx4 %4, %8, %13 sc1\n\tglobal_load_dwordx4 %5, %9, %13 sc1\n\t"
               "global_load_dwordx4 %6, %10, %13 sc1\n\tglobal_load_dwordx4 %7, %11, %13 sc1\n\ts_waitcnt vmcnt(0)"
               : "=&v"(x[0]), "=&v"(x[1]), "=&v"(x[2]), "=&v"(x[3]), "=&v"(y[0]), "=&v"(y[1]), "=&v"(y[2]), "=&v"(y[3])
               : "v"(o), "v"(o + st), "v"(o + 2 * st), "v"(o + 3 * st), "s"(base), "s"((const unsigned char*)base + 4096) : "memory");
}

#define LDSP(T, off) ((LAS T*)(L + (off)))
#define LGKM0() asm volatile("s_waitcnt lgkmcnt(0)" ::: "memory")
#define BAR() do { asm volatile("s_waitcnt lgkmcnt(0)" ::: "memory"); __builtin_amdgcn_s_barrier(); asm volatile("" ::: "memory"); } while (0)
__device__ __forceinline__ unsigned lds_arrive(LAS unsigned char* L, int c, int lane) {
  LGKM0();
  unsigned r = 0;
  if (lane == 0) r = __hip_atomic_fetch_add(LDSP(unsigned, L_CTL) + c, 1u, __ATOMIC_RELAXED, __HIP_MEMORY_SCOPE_WORKGROUP);
  r = __builtin_amdgcn_readfirstlane(r);
  asm volatile("" ::: "memory");
  return r;
}
__device__ __forceinline__ void lds_wait(LAS unsigned char* L, int c, unsigned n) {
  for (int s = 0; s < SPIN_L; ++s) {
    if (__builtin_amdgcn_readfirstlane(*(volatile LAS unsigned*)(LDSP(unsigned, L_CTL) + c)) >= n) break;
    __builtin_amdgcn_s_sleep(1);
  }
  asm volatile("" ::: "memory");
}
template <int N> __device__ __forceinline__ void wait_vm() { asm volatile("s_waitcnt vmcnt(%0)" ::"n"(N) : "memory"); }
__device__ __forceinline__ void dma_unit(unsigned lds_dst, const void* src, unsigned o0, unsigned o1, unsigned o2, unsigned o3) {
  unsigned keep;
  asm volatile("s_waitcnt lgkmcnt(0)\n\ts_mov_b32 %0, m0\n\ts_mov_b32 m0, %1\n\ts_nop 0\n\t"
               "global_load_lds_dwordx4 %3, %2 nt\n\tglobal_load_lds_dwordx4 %4, %2 offset:1024 nt\n\t"
               "global_load_lds_dwordx4 %5, %2 offset:2048 nt\n\tglobal_load_lds_dwordx4 %6, %2 offset:3072 nt\n\ts_mov_b32 m0, %0"
               : "=&s"(keep) : "s"(lds_dst), "s"(src), "v"(o0), "v"(o1), "v"(o2), "v"(o3) : "memory");
}
template <class T> __device__ __forceinline__ T ldnt(const void* p) { return __builtin_nontemporal_load((const T*)p); }
__device__ __forceinline__ void ln_stats(float s1, float s2, float& mb, float& rs) {
  const float mean = s1 * (1.f / 1024.f), var = fmaxf(s2 * (1.f / 1024.f) - mean * mean, 0.f);
  mb = rbf(mean);
  rs = __builtin_amdgcn_rcpf(rbf(__builtin_amdgcn_sqrtf(rbf(rbf(var) + 1e-5f))));
}
__device__ __forceinline__ void gemv64(const v4u (&w)[8], const float (&r)[8], int lane, LAS float* dst) {
  float acc[8] = {0, 0, 0, 0, 0, 0, 0, 0};
#pragma unroll
  for (int i = 0; i < 8; ++i)
#pragma unroll
    for (int j = 0; j < 4; ++j) { acc[2 * j] = fmaf(r[i], lo16(w[i][j]), acc[2 * j]); acc[2 * j + 1] = fmaf(r[i], hi16(w[i][j]), acc[2 * j + 1]); }
  const float c0 = tr16(tr32(acc[0], acc[4]), tr32(acc[2], acc[6])), c1 = tr16(tr32(acc[1], acc[5]), tr32(acc[3], acc[7]));
  const float d0 = c0 + dpp<ROR8>(c0), d1 = c1 + dpp<ROR8>(c1);
  dst[(lane & 7) * 8 + (lane >> 3)] = (lane & 8) ? d1 : d0;
}
constexpr unsigned L_STAMP = L_END, L_TOTAL = L_END + 8 * 64 * 4;
#define STS(i) do { const unsigned t_ = (unsigned)__builtin_amdgcn_s_memrealtime(); if (lane == 0) *LDSP(unsigned, L_STAMP + wave * 256 + (i) * 4) = t_; } while (0)

__global__ void __launch_bounds__(NTHR, 2) block_kernel(const u16* x, const unsigned char* Kc, const unsigned char* Vc, const u16* wq, const u16* wo,
                                                         const u16* w1, const u16* b1, const u16* w2, const u16* b2, const u16* cvec, const u16* wmod,
                                                         u16* out, unsigned char* ws) {
  extern __shared__ __attribute__((aligned(16))) unsigned char smem[];
  LAS unsigned char* L = (LAS unsigned char*)smem;
  const int tid = threadIdx.x, lane = tid & 63, wave = __builtin_amdgcn_readfirstlane(tid >> 6);
  const int cu = blockIdx.x, g = cu >> 6, px = (cu & 7) * 32 + (cu >> 3);
  Tag tag;
  float x1own = 0.f, gbown = 0.f, b2v = 0.f;
  float T[4];
  *LDSP(unsigned, L_STAMP + tid * 4) = 0;
  STS(0);
  if (tid < 16) LDSP(unsigned, L_CTL)[tid] = 0;

  if (wave < 4) {
    const unsigned wb = wave * 32768u, lq = lane >> 4, lc = lane & 15;
    const unsigned char* kb = Kc + ((size_t)g * 65536 + (size_t)((cu & 63) * 4 + wave) * 16) * 256;
    const unsigned char* vb = Vc + ((size_t)g * 65536 + (size_t)((cu & 63) * 4 + wave) * 16) * 256;
    const unsigned ok0 = lq * 256 + ((lc ^ lq) << 4), ok1 = ok0 ^ 64, ok2 = ok0 ^ 128, ok3 = ok0 ^ 192;
    const unsigned ov0 = lq * 256 + ((lc ^ (lq << 1)) << 4), ov1 = ov0 ^ 128;
    unsigned ka[4], va[8];
#pragma unroll
    for (int s = 0; s < 4; ++s) ka[s] = wb + lc * 256 + (((4 * s + lq) ^ lc) << 4);
    { const unsigned row = 4 * lq + (lc >> 2);
#pragma unroll
      for (int dt = 0; dt < 8; ++dt) va[dt] = wb + row * 256 + ((dt ^ (row & 7)) << 5) + ((lane & 3) << 3); }
#define ISSUE(u) do { if ((u) < 16) dma_unit(wb + ((u) & 7) * 4096, kb + ((size_t)(u) << 20), ok0, ok1, ok2, ok3); \
                      else if ((u) < 32) dma_unit(wb + ((u) & 7) * 4096, vb + ((size_t)((u) - 16) << 20), ov0, ov1, ov0, ov1); } while (0)
#define LANDED(u) do { wait_vm<4 * (((u) + LEAD - 1 < 31 ? (u) + LEAD - 1 : 31) - (u))>(); STS(16 + (u)); } while (0)
    BAR();
#pragma unroll
    for (int u = 0; u < LEAD; ++u) ISSUE(u);
    v4u kr[8][4];
#define STEP_REG(u) do { LANDED(u); ISSUE((u) + LEAD); _Pragma("unroll") for (int s = 0; s < 4; ++s) kr[u][s] = *LDSP(v4u, ka[s] + (u) * 4096); } while (0)
    STEP_REG(0); STEP_REG(1); STEP_REG(2); STEP_REG(3); STEP_REG(4); STEP_REG(5); STEP_REG(6); STEP_REG(7);
#define STEP_PACE(u) do { if ((u) + LEAD < 16) { LANDED(u); ISSUE((u) + LEAD); } } while (0)
    STEP_PACE(8); STEP_PACE(9); STEP_PACE(10); STEP_PACE(11); STEP_PACE(12); STEP_PACE(13); STEP_PACE(14);
    STS(1);
    lds_wait(L, C_Q, 4);
    STS(2);
    v8bf qf[4];
#pragma unroll
    for (int s = 0; s < 4; ++s) qf[s] = __builtin_bit_cast(v8bf, *LDSP(v4u, L_Q + (lc & 7) * 256 + (4 * s + lq) * 16));
    { const v4u tv = *LDSP(v4u, L_TAG); tag = Tag{tv.x, tv.y, tv.z, tv.w}; }
    unsigned sc[16][2];
    float mx = -INFINITY;
    auto qk = [&](int u, const v4u (&kf)[4]) {
      v4f d = {0.f, 0.f, 0.f, 0.f};
#pragma unroll
      for (int s = 0; s < 4; ++s) d = __builtin_amdgcn_mfma_f32_16x16x32_bf16(__builtin_bit_cast(v8bf, kf[s]), qf[s], d, 0, 0, 0);
      mx = fmaxf(fmaxf(mx, fmaxf(d[0], d[1])), fmaxf(d[2], d[3]));
      const unsigned a = pk2(d[0], d[1]), b = pk2(d[2], d[3]);
      sc[u][0] = pk2(lo16(a) * SCALE, hi16(a) * SCALE); sc[u][1] = pk2(lo16(b) * SCALE, hi16(b) * SCALE);
    };
#define QK_LDS(u) do { v4u kf[4]; _Pragma("unroll") for (int s = 0; s < 4; ++s) kf[s] = *LDSP(v4u, ka[s] + ((u) & 7) * 4096); qk(u, kf); } while (0)
#define STEP_K(u) do { if ((u) + LEAD >= 16) { LANDED(u); ISSUE((u) + LEAD); } QK_LDS(u); } while (0)
#pragma unroll
    for (int u = 8; u < 16 - LEAD; ++u) QK_LDS(u);
#pragma unroll
    for (int u = 0; u < 8; ++u) qk(u, kr[u]);
    if (LEAD >= 7) STEP_K(9);
    if (LEAD >= 6) STEP_K(10);
    if (LEAD >= 5) STEP_K(11);
    if (LEAD >= 4) STEP_K(12);
    if (LEAD >= 3) STEP_K(13);
    if (LEAD >= 2) STEP_K(14);
    STEP_K(15);
    STS(3);
    {
      const float m = rbf(rbf(xrow_max(mx)) * SCALE), nm = -m * LOG2E;
      float l0 = 0.f, l1 = 0.f;
#pragma unroll
      for (int u = 0; u < 16; ++u)
#pragma unroll
        for (int j = 0; j < 2; ++j) {
          const float p0 = __builtin_amdgcn_exp2f(fmaf(lo16(sc[u][j]), LOG2E, nm)), p1 = __builtin_amdgcn_exp2f(fmaf(hi16(sc[u][j]), LOG2E, nm));
          l0 += p0; l1 += p1; sc[u][j] = pk2(p0, p1);
        }
      const float l = xrow_sum(l0 + l1);
      if (lane < 8) *LDSP(v2f, L_ML + wave * 64 + lane * 8) = v2f{m, l};
      lds_arrive(L, C_ML, lane);
    }
    STS(4);
    v4f acc[8];
#pragma unroll
    for (int dt = 0; dt < 8; ++dt) acc[dt] = v4f{0.f, 0.f, 0.f, 0.f};
#define STEP_V(k) do { v4s t1[8], t2[8]; \
      LANDED(16 + 2 * (k)); ISSUE(16 + 2 * (k) + LEAD); if (16 + 2 * (k) + LEAD == 31) lds_arrive(L, C_LAST, lane); \
      _Pragma("unroll") for (int dt = 0; dt < 8; ++dt) t1[dt] = __builtin_amdgcn_ds_read_tr16_b64_v4i16(LDSP(v4s, va[dt] + ((2 * (k)) & 7) * 4096)); \
      LANDED(17 + 2 * (k)); ISSUE(17 + 2 * (k) + LEAD); if (17 + 2 * (k) + LEAD == 31) lds_arrive(L, C_LAST, lane); \
      _Pragma("unroll") for (int dt = 0; dt < 8; ++dt) t2[dt] = __builtin_amdgcn_ds_read_tr16_b64_v4i16(LDSP(v4s, va[dt] + ((2 * (k) + 1) & 7) * 4096)); \
      const v8bf pf = __builtin_bit_cast(v8bf, v4u{sc[2 * (k)][0], sc[2 * (k)][1], sc[2 * (k) + 1][0], sc[2 * (k) + 1][1]}); \
      _Pragma("unroll") for (int dt = 0; dt < 8; ++dt) \
        acc[dt] = __builtin_amdgcn_mfma_f32_16x16x32_bf16(__builtin_bit_cast(v8bf, __builtin_shufflevector(t1[dt], t2[dt], 0, 1, 2, 3, 4, 5, 6, 7)), pf, acc[dt], 0, 0, 0); \
    } while (0)
    STEP_V(0); STEP_V(1); STEP_V(2); STEP_V(3); STEP_V(4); STEP_V(5); STEP_V(6);
    lds_wait(L, C_FAC, 1);
    const float fac = *LDSP(float, L_FAC + wave * 32 + (lc & 7) * 4);
    STS(5);
    STEP_V(7);
    STS(6);
    unsigned hm = (lc & 8) ? 0xffffffffu : 0u;
    asm volatile("" : "+v"(hm));
#pragma unroll
    for (int j = 0; j < 4; ++j) {
      v4f o;
#pragma unroll
      for (int r = 0; r < 4; ++r) o[r] = asf((asu(acc[j][r]) & ~hm) | (asu(acc[4 + j][r]) & hm)) * fac;
      *LDSP(v4f, wb + (lc & 7) * 528 + (j + ((lc >> 3) << 2)) * 64 + lq * 16) = o;
    }
    BAR();
    {
      const unsigned ro = (tid >> 5) * 528 + (tid & 31) * 16;
      const v4f o = (*LDSP(v4f, ro) + *LDSP(v4f, 32768 + ro)) + (*LDSP(v4f, 65536 + ro) + *LDSP(v4f, 98304 + ro));
      g_store(ws + WS_GA, (cu * 256 + tid) * 16, o[0], o[1], o[2], o[3], tag);
    }
    STS(7);
    __builtin_amdgcn_s_sleep(DLYA);
    {
      const v4u a = g_wait1(ws + WS_GA, (tid * 256 + px) * 16, tag);
      const float o = wave_sum4(ug(a.x), ug(a.y), ug(a.z), ug(a.w));
      if (lc == 0) *LDSP(u16, L_O16 + wave * 8 + lq * 2) = (u16)pk2(o, o);
    }
    STS(8);
    BAR();
    BAR();
    BAR();
  } else {
    __builtin_amdgcn_s_setprio(2);
    const int aw = wave - 4, ta = tid - 256;
    u64 e0;
    asm volatile("global_load_dwordx2 %0, %1, %2 sc1" : "=&v"(e0) : "v"(0u), "s"(ws + WS_E) : "memory");
    const int rb = px >> 6, cb = px & 63;
    const int R0 = 256 * rb + 64 * aw, row8 = R0 + 8 * (lane >> 3);
    const v4u cr = *(const v4u*)(cvec + row8);
    v4u wm[4], wqr[8];
#pragma unroll
    for (int k = 0; k < 4; ++k) wm[k] = ldnt<v4u>(wmod + (size_t)(R0 + 4 * (lane >> 2) + k) * 6144 + 32 * cb + 8 * (lane & 3));
    const v4u xa = *(const v4u*)(x + 16 * lane), xb = *(const v4u*)(x + 16 * lane + 8);
    const unsigned xr = *(const unsigned*)(x + R0 + 2 * (lane & 31));
#pragma unroll
    for (int i = 0; i < 8; ++i) wqr[i] = ldnt<v4u>(wq + (size_t)(row8 + i) * 4096 + 64 * cb + 8 * (lane & 7));
    BAR();
    asm volatile("s_waitcnt vmcnt(16)" : "+v"(e0)::"memory");
    const u64 e1 = e0 * 6364136223846793005ull + 1442695040888963407ull;
    tag = Tag{(unsigned)(e1 >> 24) & 0x3ffu, (unsigned)(e1 >> 34) & 0x3ffu, (unsigned)(e1 >> 44) & 0x3ffu, (unsigned)(e1 >> 54) & 0x3ffu};
    const Tag2 tag2{(unsigned)(e1 >> 32), (unsigned)e1};
    float sv[8];
#pragma unroll
    for (int k = 0; k < 8; ++k) { const float c = (k & 1) ? hi16(cr[k >> 1]) : lo16(cr[k >> 1]); sv[k] = rbf(c * __builtin_amdgcn_rcpf(1.f + __builtin_amdgcn_exp2f(-LOG2E * c))); }
    {
      unsigned hm = (lane & 4) ? 0xffffffffu : 0u;
      asm volatile("" : "+v"(hm));
      float a[8] = {0, 0, 0, 0, 0, 0, 0, 0};
#pragma unroll
      for (int k = 0; k < 4; ++k) {
        const float s = asf((asu(sv[k]) & ~hm) | (asu(sv[4 + k]) & hm));
#pragma unroll
        for (int j = 0; j < 4; ++j) { a[2 * j] = fmaf(s, lo16(wm[k][j]), a[2 * j]); a[2 * j + 1] = fmaf(s, hi16(wm[k][j]), a[2 * j + 1]); }
      }
      float c0 = tr16(tr32(a[0], a[4]), tr32(a[2], a[6])), c1 = tr16(tr32(a[1], a[5]), tr32(a[3], a[7]));
      c0 += dpp<ROR8>(c0); c1 += dpp<ROR8>(c1);
      const float d0 = c0 + dpp<ROR4>(c0), d1 = c1 + dpp<ROR4>(c1);
      if (!(lane & 8)) *LDSP(float, L_P2 + aw * 128 + ((lane & 3) * 8 + (lane >> 4) * 2 + ((lane >> 2) & 1)) * 4) = (lane & 4) ? d1 : d0;
      STS(1);
      if (lds_arrive(L, C_P2, lane) == 3 && lane < 16) {
        const v2f s = (*LDSP(v2f, L_P2 + lane * 8) + *LDSP(v2f, L_P2 + 128 + lane * 8)) + (*LDSP(v2f, L_P2 + 256 + lane * 8) + *LDSP(v2f, L_P2 + 384 + lane * 8));
        g2_store(ws + WS_G1, (rb * 1024 + 16 * cb + lane) * 16, s[0], s[1], tag2);
      }
    }
    float h0, h1;
    {
      float s1 = 0.f;
#pragma unroll
      for (int j = 0; j < 4; ++j) s1 += (lo16(xa[j]) + hi16(xa[j])) + (lo16(xb[j]) + hi16(xb[j]));
      s1 = wave_sum(s1);
      const float mean = s1 * (1.f / 1024.f);
      float s2 = 0.f;
#pragma unroll
      for (int j = 0; j < 4; ++j) { float d = lo16(xa[j]) - mean; s2 = fmaf(d, d, s2); d = hi16(xa[j]) - mean; s2 = fmaf(d, d, s2);
                                    d = lo16(xb[j]) - mean; s2 = fmaf(d, d, s2); d = hi16(xb[j]) - mean; s2 = fmaf(d, d, s2); }
      s2 = wave_sum(s2);
      const float mb = rbf(mean), rs = __builtin_amdgcn_rcpf(rbf(__builtin_amdgcn_sqrtf(rbf(rbf(s2 * (1.f / 1024.f)) + 1e-5f))));
      h0 = rbf(rbf(lo16(xr) - mb) * rs); h1 = rbf(rbf(hi16(xr) - mb) * rs);
    }
    __builtin_amdgcn_s_sleep(DLY1);
    {
      v4u p[4];
      const unsigned o = ((lane >> 5) * 512 + R0 / 2 + (lane & 31)) * 16;
      for (int s = 0; s < SPIN_G; ++s) { ld4(ws + WS_G1, o, 16384, p); if (__all(g2_ok(p[0], tag2) && g2_ok(p[1], tag2) && g2_ok(p[2], tag2) && g2_ok(p[3], tag2))) break; }
      const float m0 = rbf((asf(p[0].x) + asf(p[1].x)) + (asf(p[2].x) + asf(p[3].x))), m1 = rbf((asf(p[0].z) + asf(p[1].z)) + (asf(p[2].z) + asf(p[3].z)));
      const auto r0 = __builtin_amdgcn_permlane32_swap(asu(m0), asu(m0), false, false), r1 = __builtin_amdgcn_permlane32_swap(asu(m1), asu(m1), false, false);
      h0 = rbf(rbf(h0 * rbf(1.f + asf(r0[1]))) + asf(r0[0])); h1 = rbf(rbf(h1 * rbf(1.f + asf(r1[1]))) + asf(r1[0]));
      if (lane < 32) *LDSP(v2f, L_H + aw * 256 + lane * 8) = v2f{h0, h1};
    }
    STS(2);
    {
      const v4f ha = *LDSP(v4f, L_H + aw * 256 + (lane >> 3) * 32), hb = *LDSP(v4f, L_H + aw * 256 + (lane >> 3) * 32 + 16);
      const float hv[8] = {ha[0], ha[1], ha[2], ha[3], hb[0], hb[1], hb[2], hb[3]};
      gemv64(wqr, hv, lane, LDSP(float, L_P3 + aw * 256));
    }
    if (lds_arrive(L, C_P3, lane) == 3) {
      if (lane < 32) {
        const v2f s = (*LDSP(v2f, L_P3 + lane * 8) + *LDSP(v2f, L_P3 + 256 + lane * 8)) + (*LDSP(v2f, L_P3 + 512 + lane * 8) + *LDSP(v2f, L_P3 + 768 + lane * 8));
        g2_store(ws + WS_G2, (rb * 2048 + 32 * cb + lane) * 16, s[0], s[1], tag2);
      }
    }
    STS(3);
    __builtin_amdgcn_s_sleep(DLY2);
    {
      v4u p[4], q[4];
      const unsigned o = (g * 512 + ta) * 16;
      for (int s = 0; s < SPIN_G; ++s) {
        ld8(ws + WS_G2, o, 32768, p, q);
        if (__all(g2_ok(p[0], tag2) && g2_ok(p[1], tag2) && g2_ok(p[2], tag2) && g2_ok(p[3], tag2) && g2_ok(q[0], tag2) && g2_ok(q[1], tag2) && g2_ok(q[2], tag2) && g2_ok(q[3], tag2))) break;
      }
      *LDSP(unsigned, L_Q + ta * 4) = pk2((asf(p[0].x) + asf(p[1].x)) + (asf(p[2].x) + asf(p[3].x)), (asf(p[0].z) + asf(p[1].z)) + (asf(p[2].z) + asf(p[3].z)));
      *LDSP(unsigned, L_Q + 1024 + ta * 4) = pk2((asf(q[0].x) + asf(q[1].x)) + (asf(q[2].x) + asf(q[3].x)), (asf(q[0].z) + asf(q[1].z)) + (asf(q[2].z) + asf(q[3].z)));
      if (ta == 0) *LDSP(v4u, L_TAG) = v4u{tag.a, tag.b, tag.c, tag.d};
      lds_arrive(L, C_Q, lane);
    }
    STS(4);
    {
      v4u wr[8];
#pragma unroll
      for (int i = 0; i < 8; ++i) wr[i] = ldnt<v4u>(wmod + (size_t)(row8 + i) * 6144 + 2048 + 64 * cb + 8 * (lane & 7));
      v4u pr[6];
#pragma unroll
      for (int k = 0; k < 2; ++k) { const int ch = min(ta + 256 * k, 447);
        pr[k] = *(const v4u*)(w1 + 8 * ch); pr[2 + k] = *(const v4u*)(b1 + 8 * ch); pr[4 + k] = *(const v4u*)(w2 + 8 * ch); }
      gemv64(wr, sv, lane, LDSP(float, L_P4 + aw * 256));
      if (lds_arrive(L, C_P4, lane) == 3 && lane < 16) {
        const v4f s = (*LDSP(v4f, L_P4 + lane * 16) + *LDSP(v4f, L_P4 + 256 + lane * 16)) + (*LDSP(v4f, L_P4 + 512 + lane * 16) + *LDSP(v4f, L_P4 + 768 + lane * 16));
        g_store(ws + WS_G3, (rb * 1024 + 16 * cb + lane) * 16, s[0], s[1], s[2], s[3], tag);
      }
#pragma unroll
      for (int k = 0; k < 2; ++k) { const int ch = min(ta + 256 * k, 447);
        *LDSP(v4u, L_PRM + ch * 16) = pr[k]; *LDSP(v4u, L_PRM + 7168 + ch * 16) = pr[2 + k]; *LDSP(v4u, L_PRM + 14336 + ch * 16) = pr[4 + k]; }
    }
    STS(5);
    lds_wait(L, C_ML, 4);
    STS(6);
    if (aw == 0) {
      const int h = 2 * (lane & 3);
      float mw[4][2], lw[4][2];
#pragma unroll
      for (int w = 0; w < 4; ++w) { const v4f v = *LDSP(v4f, L_ML + w * 64 + h * 8); mw[w][0] = v[0]; lw[w][0] = v[1]; mw[w][1] = v[2]; lw[w][1] = v[3]; }
      float M[2], S[2];
#pragma unroll
      for (int j = 0; j < 2; ++j) {
        M[j] = fmaxf(fmaxf(mw[0][j], mw[1][j]), fmaxf(mw[2][j], mw[3][j])); S[j] = 0.f;
#pragma unroll
        for (int w = 0; w < 4; ++w) S[j] = fmaf(lw[w][j], __builtin_amdgcn_exp2f((mw[w][j] - M[j]) * LOG2E), S[j]);
      }
      if (lane < 4) g_store(ws + WS_GM, (cu * 4 + lane) * 16, M[0], S[0], M[1], S[1], tag);
      __builtin_amdgcn_s_sleep(100);
      v4u r[4];
      const unsigned o = (g * 256 + lane) * 16;
      g_wait4(ws + WS_GM, o, o + 1024, o + 2048, o + 3072, tag, r[0], r[1], r[2], r[3]);
#pragma unroll
      for (int j = 0; j < 2; ++j) {
        float m = fmaxf(fmaxf(ug(r[0][2 * j]), ug(r[1][2 * j])), fmaxf(ug(r[2][2 * j]), ug(r[3][2 * j])));
        m = fmaxf(m, dpp<ROR8>(m)); m = fmaxf(m, dpp<ROR4>(m)); m = xrow_max(m);
        float s = 0.f;
#pragma unroll
        for (int k = 0; k < 4; ++k) s = fmaf(ug(r[k][2 * j + 1]), __builtin_amdgcn_exp2f((ug(r[k][2 * j]) - m) * LOG2E), s);
        s += dpp<ROR8>(s); s += dpp<ROR4>(s); s = xrow_sum(s);
        const float inv = 1.f / s;
        if (lane < 4) {
#pragma unroll
          for (int w = 0; w < 4; ++w) *LDSP(float, L_FAC + w * 32 + (h + j) * 4) = __builtin_amdgcn_exp2f((mw[w][j] - m) * LOG2E) * inv;
        }
      }
      lds_arrive(L, C_FAC, lane);
    }
    float c1[4], sb[4], gaown;
    {
      v4u p[4], q[4], u[4], v[4];
      for (int s = 0; s < SPIN_G; ++s) {
        ld8(ws + WS_G3, (256 + ta) * 16, 16384, p, q);
        if (__all(g_ok(p[0], tag) && g_ok(p[1], tag) && g_ok(p[2], tag) && g_ok(p[3], tag) && g_ok(q[0], tag) && g_ok(q[1], tag) && g_ok(q[2], tag) && g_ok(q[3], tag))) break;
        __builtin_amdgcn_s_sleep(16);
      }
#pragma unroll
      for (int k = 0; k < 4; ++k) {
        sb[k] = rbf((ug(p[0][k]) + ug(p[1][k])) + (ug(p[2][k]) + ug(p[3][k])));
        c1[k] = rbf(1.f + rbf((ug(q[0][k]) + ug(q[1][k])) + (ug(q[2][k]) + ug(q[3][k]))));
      }
      g_wait4(ws + WS_G3, px * 16, 16384 + px * 16, 32768 + px * 16, 49152 + px * 16, tag, u[0], u[1], u[2], u[3]);
      g_wait4(ws + WS_G3, 12288 + px * 16, 28672 + px * 16, 45056 + px * 16, 61440 + px * 16, tag, v[0], v[1], v[2], v[3]);
      const int c = lane & 3;
      float ga4[4], gb4[4];
#pragma unroll
      for (int k = 0; k < 4; ++k) { ga4[k] = (ug(u[0][k]) + ug(u[1][k])) + (ug(u[2][k]) + ug(u[3][k])); gb4[k] = (ug(v[0][k]) + ug(v[1][k])) + (ug(v[2][k]) + ug(v[3][k])); }
      gaown = rbf(c == 0 ? ga4[0] : c == 1 ? ga4[1] : c == 2 ? ga4[2] : ga4[3]);
      gbown = rbf(c == 0 ? gb4[0] : c == 1 ? gb4[1] : c == 2 ? gb4[2] : gb4[3]);
      x1own = lo16(x[4 * px + c]);
      b2v = lo16(b2[0]);
    }
    STS(7);
    lds_wait(L, C_LAST, 4);
    v2u wor[16];
#pragma unroll
    for (int r = 0; r < 16; ++r) wor[r] = ldnt<v2u>(wo + (size_t)((r >> 2) * 1024 + 4 * px + (r & 3)) * 1024 + 4 * ta);
    BAR();
    unsigned wp[8][4];
#pragma unroll
    for (int p = 0; p < 8; ++p) {
      asm volatile("" : "+v"(wor[2 * p]), "+v"(wor[2 * p + 1]));
      wp[p][0] = __builtin_amdgcn_perm(wor[2 * p + 1].x, wor[2 * p].x, 0x05040100u); wp[p][1] = __builtin_amdgcn_perm(wor[2 * p + 1].x, wor[2 * p].x, 0x07060302u);
      wp[p][2] = __builtin_amdgcn_perm(wor[2 * p + 1].y, wor[2 * p].y, 0x05040100u); wp[p][3] = __builtin_amdgcn_perm(wor[2 * p + 1].y, wor[2 * p].y, 0x07060302u);
      asm volatile("" : "+v"(wp[p][0]), "+v"(wp[p][1]), "+v"(wp[p][2]), "+v"(wp[p][3]));
    }
    STS(8);
    BAR();
    {
      const v4u oa = *LDSP(v4u, L_O16), ob = *LDSP(v4u, L_O16 + 16);
      float a[4] = {0.f, 0.f, 0.f, 0.f};
#pragma unroll
      for (int p = 0; p < 8; ++p)
#pragma unroll
        for (int c = 0; c < 4; ++c)
          a[c] = __builtin_amdgcn_fdot2_f32_bf16(__builtin_bit_cast(v2bf, wp[p][c]), __builtin_bit_cast(v2bf, p < 4 ? oa[p] : ob[p - 4]), a[c], false);
      g_store(ws + WS_GB, (cu * 256 + ta) * 16, a[0], a[1], a[2], a[3], tag);
    }
    STS(9);
    __builtin_amdgcn_s_sleep(DLYB);
    {
      const v4u a = g_wait1(ws + WS_GB, (ta * 256 + px) * 16, tag);
      const float s = wave_sum4(ug(a.x), ug(a.y), ug(a.z), ug(a.w));
      if ((lane & 15) == 0) *LDSP(float, L_RB + (lane >> 4) * 16 + aw * 4) = s;
    }
    STS(10);
    BAR();
    if (aw == 0) {
      const v4f s = *LDSP(v4f, L_RB + (lane & 3) * 16);
      x1own = rbf(x1own + rbf(gaown * rbf((s[0] + s[1]) + (s[2] + s[3]))));
      const float a0 = dpp<0x00>(x1own), a1 = dpp<0x55>(x1own), a2 = dpp<0xAA>(x1own), a3 = dpp<0xFF>(x1own);
      if (lane == 0) g_store(ws + WS_GC, px * 16, a0, a1, a2, a3, tag);
    }
    STS(11);
    __builtin_amdgcn_s_sleep(DLYC);
    float h2[4];
    {
      const v4u a = g_wait1(ws + WS_GC, ta * 16, tag);
      if (cu == 0 && ta == 0) asm volatile("global_store_dwordx2 %0, %1, %2 sc1" ::"v"(0u), "v"(e1), "s"(ws + WS_E) : "memory");
      STS(12);
#pragma unroll
      for (int k = 0; k < 4; ++k) h2[k] = ug(a[k]);
      const float u = tr32((h2[0] + h2[1]) + (h2[2] + h2[3]), fmaf(h2[0], h2[0], h2[1] * h2[1]) + fmaf(h2[2], h2[2], h2[3] * h2[3]));
      const float s = row_sum(tr16(u, u));
      if ((lane & 31) == 0) *LDSP(float, L_R1 + (lane >> 5) * 16 + aw * 4) = s;
    }
    BAR();
    {
      const v4f p = *LDSP(v4f, L_R1), q = *LDSP(v4f, L_R1 + 16);
      float mb, rs;
      ln_stats((p[0] + p[1]) + (p[2] + p[3]), (q[0] + q[1]) + (q[2] + q[3]), mb, rs);
#pragma unroll
      for (int k = 0; k < 4; ++k) h2[k] = rbf(rbf(rbf(rbf(h2[k] - mb) * rs) * c1[k]) + sb[k]);
      const float u = tr32((h2[0] + h2[1]) + (h2[2] + h2[3]), fmaf(h2[0], h2[0], h2[1] * h2[1]) + fmaf(h2[2], h2[2], h2[3] * h2[3]));
      const float s = row_sum(tr16(u, u));
      if ((lane & 31) == 0) *LDSP(float, L_R2 + (lane >> 5) * 16 + aw * 4) = s;
      if (ta == px) *LDSP(v4f, L_H2) = v4f{h2[0], h2[1], h2[2], h2[3]};
    }
  }
  float pw1[7], pb1[7], pw2[7];
#pragma unroll
  for (int k = 0; k < 7; ++k) {
    pw1[k] = lo16(*LDSP(u16, L_PRM + (tid + 512 * k) * 2)); pb1[k] = lo16(*LDSP(u16, L_PRM + 7168 + (tid + 512 * k) * 2));
    pw2[k] = lo16(*LDSP(u16, L_PRM + 14336 + (tid + 512 * k) * 2));
  }
  BAR();
  {
    const v4f p = *LDSP(v4f, L_R2), q = *LDSP(v4f, L_R2 + 16), h = *LDSP(v4f, L_H2);
    float mb, rs;
    ln_stats((p[0] + p[1]) + (p[2] + p[3]), (q[0] + q[1]) + (q[2] + q[3]), mb, rs);
#pragma unroll
    for (int k = 0; k < 4; ++k) T[k] = rbf(rbf(h[k] - mb) * rs);
  }
  STS(13);
  {
    float y[4] = {0.f, 0.f, 0.f, 0.f};
    constexpr float G0 = -2.f * 0.7978845608028654f * LOG2E, G1 = G0 * 0.044715f;
#pragma unroll
    for (int k = 0; k < 7; ++k)
#pragma unroll
      for (int c = 0; c < 4; c += 2) {
        const unsigned a = pk2(T[c] * pw1[k], T[c + 1] * pw1[k]);
        const unsigned u = pk2(lo16(a) + pb1[k], hi16(a) + pb1[k]);
        const float u0 = lo16(u), u1 = hi16(u);
        const float e0 = __builtin_amdgcn_exp2f(u0 * fmaf(u0 * u0, G1, G0)), e1 = __builtin_amdgcn_exp2f(u1 * fmaf(u1 * u1, G1, G0));
        const unsigned gl = pk2(u0 * __builtin_amdgcn_rcpf(1.f + e0), u1 * __builtin_amdgcn_rcpf(1.f + e1));
        y[c] = fmaf(lo16(gl), pw2[k], y[c]); y[c + 1] = fmaf(hi16(gl), pw2[k], y[c + 1]);
      }
    const float s = wave_sum4(y[0], y[1], y[2], y[3]);
    if ((lane & 15) == 0) *LDSP(float, L_R3 + (lane >> 4) * 32 + wave * 4) = s;
  }
  STS(14);
  BAR();
  if (wave == 4 && lane < 4) {
    const v4f p = *LDSP(v4f, L_R3 + lane * 32), q = *LDSP(v4f, L_R3 + lane * 32 + 16);
    const float y = rbf(rbf(((p[0] + p[1]) + (p[2] + p[3])) + ((q[0] + q[1]) + (q[2] + q[3]))) + b2v);
    const float o = x1own + rbf(gbown * y);
    out[4 * px + lane] = (u16)pk2(o, o);
  }
  STS(15);
  LGKM0();
  ((unsigned*)(ws + WS_ST))[cu * 512 + tid] = *LDSP(unsigned, L_STAMP + tid * 4);
}
__global__ void probe_kernel(const unsigned* hs) {
  __shared__ unsigned smn[256], smx[256], ssum[256], st0[256];
  const int t = threadIdx.x;
  const unsigned tb = hs[0] - 100000u;
  unsigned t0 = ~0u;
  for (int w = 0; w < 8; ++w) t0 = min(t0, hs[(t * 8 + w) * 64] - tb);
  st0[t] = t0; __syncthreads();
  for (int i = 0; i < 256; ++i) t0 = min(t0, st0[i]);
  unsigned mn = ~0u, mx = 0, sum = 0;
  for (int w = 0 * 4; w < 0 * 4 + 4; ++w) { unsigned v = hs[(t * 8 + w) * 64 + 0] - tb - t0; mn = min(mn, v); mx = max(mx, v); sum += v; }
  smn[t] = mn; smx[t] = mx; ssum[t] = sum; __syncthreads();
  if (t == 0) {
    for (int i = 1; i < 256; ++i) { mn = min(mn, smn[i]); mx = max(mx, smx[i]); sum += ssum[i]; }
    unsigned long long val = mx;
    if (val > 20000) val = 20000;
    const unsigned long long a = __builtin_amdgcn_s_memrealtime();
    while (__builtin_amdgcn_s_memrealtime() - a < val * 100) __builtin_amdgcn_s_sleep(8);
  }
}
}

extern "C" void kernel_launch(void* const* d_in, const int* in_sizes, int n_in, void* d_out, int out_size, void* d_ws, size_t ws_size, hipStream_t stream) {
  (void)in_sizes; (void)n_in; (void)out_size; (void)ws_size;
  (void)hipFuncSetAttribute((const void*)block_kernel, hipFuncAttributeMaxDynamicSharedMemorySize, L_TOTAL);
  hipLaunchKernelGGL(block_kernel, dim3(NCU), dim3(NTHR), L_TOTAL, stream, (const u16*)d_in[0], (const unsigned char*)d_in[1], (const unsigned char*)d_in[2],
                     (const u16*)d_in[3], (const u16*)d_in[4], (const u16*)d_in[6], (const u16*)d_in[7], (const u16*)d_in[8], (const u16*)d_in[9],
                     (const u16*)d_in[10], (const u16*)d_in[11], (u16*)d_out, (unsigned char*)d_ws);
  hipLaunchKernelGGL(probe_kernel, dim3(1), dim3(256), 0, stream, (const unsigned*)((char*)d_ws + WS_ST));
}
```

```cpp
#include <hip/hip_runtime.h>
#include <hip/hip_bf16.h>
#include <stdint.h>

#define STAMP 1
#define LEAD 3
#define DLY1 44
#define DLY2 40
#define DLYA 14
#define DLYB 14
#define DLYC 14

namespace {
#define LAS __attribute__((address_space(3)))
typedef unsigned short u16;
typedef unsigned long long u64;
typedef unsigned v4u __attribute__((ext_vector_type(4)));
typedef unsigned v2u __attribute__((ext_vector_type(2)));
typedef float v4f __attribute__((ext_vector_type(4)));
typedef float v2f __attribute__((ext_vector_type(2)));
typedef short v4s __attribute__((ext_vector_type(4)));
typedef __bf16 v8bf __attribute__((ext_vector_type(8)));
typedef __bf16 v2bf __attribute__((ext_vector_type(2)));

constexpr int NCU = 256, NTHR = 512;
constexpr unsigned WS_E = 0, WS_GC = 4096, WS_GM = 16384, WS_G1 = 65536, WS_G3 = WS_G1 + 65536, WS_G2 = WS_G3 + 65536;
constexpr unsigned WS_GA = 1u << 20, WS_GB = 2u << 20, WS_ST = 3u << 20;
constexpr unsigned L_PRM = 131072, L_Q = L_PRM + 21504, L_P2 = L_Q + 2048, L_P3 = L_P2 + 512, L_P4 = L_P3 + 1024, L_H = L_P4 + 1024, L_ML = L_H + 1024, L_FAC = L_ML + 256,
                   L_O16 = L_FAC + 128, L_RB = L_O16 + 64, L_R1 = L_RB + 64, L_R2 = L_R1 + 32, L_H2 = L_R2 + 32, L_R3 = L_H2 + 16, L_TAG = L_R3 + 128,
                   L_CTL = L_TAG + 16, L_END = L_CTL + 64;
enum { C_P2 = 0, C_P3 = 1, C_Q = 2, C_P4 = 3, C_ML = 4, C_FAC = 5, C_LAST = 6 };
constexpr int SPIN_G = 1 << 17, SPIN_L = 1 << 22;
constexpr float LOG2E = 1.4426950408889634f, SCALE = 0.08838834764831845f;

struct Tag { unsigned a, b, c, d; };
struct Tag2 { unsigned a, b; };

__device__ __forceinline__ float asf(unsigned u) { return __builtin_bit_cast(float, u); }
__device__ __forceinline__ unsigned asu(float f) { return __builtin_bit_cast(unsigned, f); }
__device__ __forceinline__ unsigned pk2(float lo, float hi) { v2f p = {lo, hi}; return __builtin_bit_cast(unsigned, __builtin_convertvector(p, v2bf)); }
__device__ __forceinline__ float lo16(unsigned u) { return asf(u << 16); }
__device__ __forceinline__ float hi16(unsigned u) { return asf(u & 0xffff0000u); }
__device__ __forceinline__ float rbf(float f) { return lo16(pk2(f, f)); }
template <int C> __device__ __forceinline__ float dpp(float x) { return asf((unsigned)__builtin_amdgcn_mov_dpp((int)asu(x), C, 0xf, 0xf, true)); }
constexpr int XOR1 = 0xB1, XOR2 = 0x4E, HMIR = 0x141, ROR8 = 0x128, ROR4 = 0x124, ROR2 = 0x122, ROR1 = 0x121;
__device__ __forceinline__ float tr32(float a, float b) { auto r = __builtin_amdgcn_permlane32_swap(asu(a), asu(b), false, false); return asf(r[0]) + asf(r[1]); }
__device__ __forceinline__ float tr16(float a, float b) { auto r = __builtin_amdgcn_permlane16_swap(asu(a), asu(b), false, false); return asf(r[0]) + asf(r[1]); }
__device__ __forceinline__ float row_sum(float x) { x += dpp<ROR8>(x); x += dpp<ROR4>(x); x += dpp<ROR2>(x); x += dpp<ROR1>(x); return x; }
__device__ __forceinline__ float wave_sum(float x) { return row_sum(tr16(tr32(x, x), tr32(x, x))); }
__device__ __forceinline__ float wave_sum4(float a, float b, float c, float d) { return row_sum(tr16(tr32(a, c), tr32(b, d))); }
__device__ __forceinline__ float xrow_max(float x) {
  auto s = __builtin_amdgcn_permlane16_swap(asu(x), asu(x), false, false); x = fmaxf(asf(s[0]), asf(s[1]));
  auto t = __builtin_amdgcn_permlane32_swap(asu(x), asu(x), false, false); return fmaxf(asf(t[0]), asf(t[1]));
}
__device__ __forceinline__ float xrow_sum(float x) { x = tr16(x, x); return tr32(x, x); }

__device__ __forceinline__ unsigned tg(float v, unsigned t) { return ((asu(v) + 0x200u) & ~0x3ffu) | t; }
__device__ __forceinline__ float ug(unsigned v) { return asf(v & ~0x3ffu); }
__device__ __forceinline__ void g_store(const void* base, unsigned off, float a, float b, float c, float d, const Tag& t) {
  v4u v = {tg(a, t.a), tg(b, t.b), tg(c, t.c), tg(d, t.d)};
  asm volatile("global_store_dwordx4 %0, %1, %2 sc1\n\ts_nop 1" ::"v"(off), "v"(v), "s"(base) : "memory");
}
__device__ __forceinline__ bool g_ok(const v4u& x, const Tag& t) { return (((x.x ^ t.a) | (x.y ^ t.b) | (x.z ^ t.c) | (x.w ^ t.d)) & 0x3ffu) == 0u; }
__device__ __forceinline__ v4u g_wait1(const void* base, unsigned off, const Tag& t) {
  v4u x;
  for (int s = 0; s < SPIN_G; ++s) {
    asm volatile("global_load_dwordx4 %0, %1, %2 sc1\n\ts_waitcnt vmcnt(0)" : "=&v"(x) : "v"(off), "s"(base) : "memory");
    if (__all(g_ok(x, t))) break;
  }
  return x;
}
__device__ __forceinline__ void g_wait2(const void* base, unsigned o0, unsigned o1, const Tag& t, v4u& x0, v4u& x1, int nap) {
  for (int s = 0; s < SPIN_G; ++s) {
    asm volatile("global_load_dwordx4 %0, %2, %4 sc1\n\tglobal_load_dwordx4 %1, %3, %4 sc1\n\ts_waitcnt vmcnt(0)"
                 : "=&v"(x0), "=&v"(x1) : "v"(o0), "v"(o1), "s"(base) : "memory");
    if (__all(g_ok(x0, t) && g_ok(x1, t))) break;
    if (nap) __builtin_amdgcn_s_sleep(16);
  }
}
__device__ __forceinline__ void g_wait4(const void* base, unsigned o0, unsigned o1, unsigned o2, unsigned o3, const Tag& t, v4u& x0, v4u& x1, v4u& x2, v4u& x3) {
  for (int s = 0; s < SPIN_G; ++s) {
    asm volatile("global_load_dwordx4 %0, %4, %8 sc1\n\tglobal_load_dwordx4 %1, %5, %8 sc1\n\tglobal_load_dwordx4 %2, %6, %8 sc1\n\t"
                 "global_load_dwordx4 %3, %7, %8 sc1\n\ts_waitcnt vmcnt(0)"
                 : "=&v"(x0), "=&v"(x1), "=&v"(x2), "=&v"(x3) : "v"(o0), "v"(o1), "v"(o2), "v"(o3), "s"(base) : "memory");
    if (__all(g_ok(x0, t) && g_ok(x1, t) && g_ok(x2, t) && g_ok(x3, t))) break;
    __builtin_amdgcn_s_sleep(16);
  }
}

__device__ __forceinline__ void g2_store(const void* base, unsigned off, float a, float b, const Tag2& t) {
  v4u v = {asu(a), t.a, asu(b), t.b};
  asm volatile("global_store_dwordx4 %0, %1, %2 sc1\n\ts_nop 1" ::"v"(off), "v"(v), "s"(base) : "memory");
}
__device__ __forceinline__ bool g2_ok(const v4u& x, const Tag2& t) { return ((x.y ^ t.a) | (x.w ^ t.b)) == 0u; }
__device__ __forceinline__ void ld4(const void* base, unsigned o, unsigned st, v4u (&x)[4]) {
  asm volatile("global_load_dwordx4 %0, %4, %8 sc1\n\tglobal_load_dwordx4 %1, %5, %8 sc1\n\tglobal_load_dwordx4 %2, %6, %8 sc1\n\t"
               "global_load_dwordx4 %3, %7, %8 sc1\n\ts_waitcnt vmcnt(0)"
               : "=&v"(x[0]), "=&v"(x[1]), "=&v"(x[2]), "=&v"(x[3]) : "v"(o), "v"(o + st), "v"(o + 2 * st), "v"(o + 3 * st), "s"(base) : "memory");
}
__device__ __forceinline__ void ld8(const void* base, unsigned o, unsigned st, v4u (&x)[4], v4u (&y)[4]) {
  asm volatile("global_load_dwordx4 %0, %8, %12 sc1\n\tglobal_load_dwordx4 %1, %9, %12 sc1\n\tglobal_load_dwordx4 %2, %10, %12 sc1\n\t"
               "global_load_dwordx4 %3, %11, %12 sc1\n\tglobal_load_dwordx4 %4, %8, %13 sc1\n\tglobal_load_dwordx4 %5, %9, %13 sc1\n\t"
               "global_load_dwordx4 %6, %10, %13 sc1\n\tglobal_load_dwordx4 %7, %11, %13 sc1\n\ts_waitcnt vmcnt(0)"
               : "=&v"(x[0]), "=&v"(x[1]), "=&v"(x[2]), "=&v"(x[3]), "=&v"(y[0]), "=&v"(y[1]), "=&v"(y[2]), "=&v"(y[3])
               : "v"(o), "v"(o + st), "v"(o + 2 * st), "v"(o + 3 * st), "s"(base), "s"((const unsigned char*)base + 4096) : "memory");
}

#define LDSP(T, off) ((LAS T*)(L + (off)))
#define LGKM0() asm volatile("s_waitcnt lgkmcnt(0)" ::: "memory")
#define BAR() do { asm volatile("s_waitcnt lgkmcnt(0)" ::: "memory"); __builtin_amdgcn_s_barrier(); asm volatile("" ::: "memory"); } while (0)
__device__ __forceinline__ unsigned lds_arrive(LAS unsigned char* L, int c, int lane) {
  LGKM0();
  unsigned r = 0;
  if (lane == 0) r = __hip_atomic_fetch_add(LDSP(unsigned, L_CTL) + c, 1u, __ATOMIC_RELAXED, __HIP_MEMORY_SCOPE_WORKGROUP);
  r = __builtin_amdgcn_readfirstlane(r);
  asm volatile("" ::: "memory");
  return r;
}
__device__ __forceinline__ void lds_wait(LAS unsigned char* L, int c, unsigned n) {
  for (int s = 0; s < SPIN_L; ++s) {
    if (__builtin_amdgcn_readfirstlane(*(volatile LAS unsigned*)(LDSP(unsigned, L_CTL) + c)) >= n) break;
    __builtin_amdgcn_s_sleep(1);
  }
  asm volatile("" ::: "memory");
}
template <int N> __device__ __forceinline__ void wait_vm() { asm volatile("s_waitcnt vmcnt(%0)" ::"n"(N) : "memory"); }
__device__ __forceinline__ void dma_unit(unsigned lds_dst, const void* src, unsigned o0, unsigned o1, unsigned o2, unsigned o3) {
  unsigned keep;
  asm volatile("s_waitcnt lgkmcnt(0)\n\ts_mov_b32 %0, m0\n\ts_mov_b32 m0, %1\n\ts_nop 0\n\t"
               "global_load_lds_dwordx4 %3, %2 nt\n\tglobal_load_lds_dwordx4 %4, %2 offset:1024 nt\n\t"
               "global_load_lds_dwordx4 %5, %2 offset:2048 nt\n\tglobal_load_lds_dwordx4 %6, %2 offset:3072 nt\n\ts_mov_b32 m0, %0"
               : "=&s"(keep) : "s"(lds_dst), "s"(src), "v"(o0), "v"(o1), "v"(o2), "v"(o3) : "memory");
}
template <class T> __device__ __forceinline__ T ldnt(const void* p) { return __builtin_nontemporal_load((const T*)p); }
__device__ __forceinline__ void ln_stats(float s1, float s2, float& mb, float& rs) {
  const float mean = s1 * (1.f / 1024.f), var = fmaxf(s2 * (1.f / 1024.f) - mean * mean, 0.f);
  mb = rbf(mean);
  rs = __builtin_amdgcn_rcpf(rbf(__builtin_amdgcn_sqrtf(rbf(rbf(var) + 1e-5f))));
}
__device__ __forceinline__ void gemv64(const v4u (&w)[8], const float (&r)[8], int lane, LAS float* dst) {
  float acc[8] = {0, 0, 0, 0, 0, 0, 0, 0};
#pragma unroll
  for (int i = 0; i < 8; ++i)
#pragma unroll
    for (int j = 0; j < 4; ++j) { acc[2 * j] = fmaf(r[i], lo16(w[i][j]), acc[2 * j]); acc[2 * j + 1] = fmaf(r[i], hi16(w[i][j]), acc[2 * j + 1]); }
  const float c0 = tr16(tr32(acc[0], acc[4]), tr32(acc[2], acc[6])), c1 = tr16(tr32(acc[1], acc[5]), tr32(acc[3], acc[7]));
  const float d0 = c0 + dpp<ROR8>(c0), d1 = c1 + dpp<ROR8>(c1);
  dst[(lane & 7) * 8 + (lane >> 3)] = (lane & 8) ? d1 : d0;
}
constexpr unsigned L_STAMP = L_END, L_TOTAL = L_END + 8 * 64 * 4;
#define STS(i) do { const unsigned t_ = (unsigned)__builtin_amdgcn_s_memrealtime(); if (lane == 0) *LDSP(unsigned, L_STAMP + wave * 256 + (i) * 4) = t_; } while (0)

__global__ void __launch_bounds__(NTHR, 2) block_kernel(const u16* x, const unsigned char* Kc, const unsigned char* Vc, const u16* wq, const u16* wo,
                                                         const u16* w1, const u16* b1, const u16* w2, const u16* b2, const u16* cvec, const u16* wmod,
                                                         u16* out, unsigned char* ws) {
  extern __shared__ __attribute__((aligned(16))) unsigned char smem[];
  LAS unsigned char* L = (LAS unsigned char*)smem;
  const int tid = threadIdx.x, lane = tid & 63, wave = __builtin_amdgcn_readfirstlane(tid >> 6);
  const int cu = blockIdx.x, g = cu >> 6, px = (cu & 7) * 32 + (cu >> 3);
  Tag tag;
  float x1own = 0.f, gbown = 0.f, b2v = 0.f;
  float T[4];
  *LDSP(unsigned, L_STAMP + tid * 4) = 0;
  STS(0);
  if (tid < 16) LDSP(unsigned, L_CTL)[tid] = 0;

  if (wave < 4) {
    const unsigned wb = wave * 32768u, lq = lane >> 4, lc = lane & 15;
    const unsigned char* kb = Kc + ((size_t)g * 65536 + (size_t)((cu & 63) * 4 + wave) * 16) * 256;
    const unsigned char* vb = Vc + ((size_t)g * 65536 + (size_t)((cu & 63) * 4 + wave) * 16) * 256;
    const unsigned ok0 = lq * 256 + ((lc ^ lq) << 4), ok1 = ok0 ^ 64, ok2 = ok0 ^ 128, ok3 = ok0 ^ 192;
    const unsigned ov0 = lq * 256 + ((lc ^ (lq << 1)) << 4), ov1 = ov0 ^ 128;
    unsigned ka[4], va[8];
#pragma unroll
    for (int s = 0; s < 4; ++s) ka[s] = wb + lc * 256 + (((4 * s + lq) ^ lc) << 4);
    { const unsigned row = 4 * lq + (lc >> 2);
#pragma unroll
      for (int dt = 0; dt < 8; ++dt) va[dt] = wb + row * 256 + ((dt ^ (row & 7)) << 5) + ((lane & 3) << 3); }
#define ISSUE(u) do { if ((u) < 16) dma_unit(wb + ((u) & 7) * 4096, kb + ((size_t)(u) << 20), ok0, ok1, ok2, ok3); \
                      else if ((u) < 32) dma_unit(wb + ((u) & 7) * 4096, vb + ((size_t)((u) - 16) << 20), ov0, ov1, ov0, ov1); } while (0)
#define LANDED(u) do { wait_vm<4 * (((u) + LEAD - 1 < 31 ? (u) + LEAD - 1 : 31) - (u))>(); STS(16 + (u)); } while (0)
    BAR();
#pragma unroll
    for (int u = 0; u < LEAD; ++u) ISSUE(u);
    v4u kr[8][4];
#define STEP_REG(u) do { LANDED(u); ISSUE((u) + LEAD); _Pragma("unroll") for (int s = 0; s < 4; ++s) kr[u][s] = *LDSP(v4u, ka[s] + (u) * 4096); } while (0)
    STEP_REG(0); STEP_REG(1); STEP_REG(2); STEP_REG(3); STEP_REG(4); STEP_REG(5); STEP_REG(6); STEP_REG(7);
#define STEP_PACE(u) do { if ((u) + LEAD < 16) { LANDED(u); ISSUE((u) + LEAD); } } while (0)
    STEP_PACE(8); STEP_PACE(9); STEP_PACE(10); STEP_PACE(11); STEP_PACE(12); STEP_PACE(13); STEP_PACE(14);
    STS(1);
    lds_wait(L, C_Q, 4);
    STS(2);
    v8bf qf[4];
#pragma unroll
    for (int s = 0; s < 4; ++s) qf[s] = __builtin_bit_cast(v8bf, *LDSP(v4u, L_Q + (lc & 7) * 256 + (4 * s + lq) * 16));
    { const v4u tv = *LDSP(v4u, L_TAG); tag = Tag{tv.x, tv.y, tv.z, tv.w}; }
    unsigned sc[16][2];
    float mx = -INFINITY;
    auto qk = [&](int u, const v4u (&kf)[4]) {
      v4f d = {0.f, 0.f, 0.f, 0.f};
#pragma unroll
      for (int s = 0; s < 4; ++s) d = __builtin_amdgcn_mfma_f32_16x16x32_bf16(__builtin_bit_cast(v8bf, kf[s]), qf[s], d, 0, 0, 0);
      mx = fmaxf(fmaxf(mx, fmaxf(d[0], d[1])), fmaxf(d[2], d[3]));
      const unsigned a = pk2(d[0], d[1]), b = pk2(d[2], d[3]);
      sc[u][0] = pk2(lo16(a) * SCALE, hi16(a) * SCALE); sc[u][1] = pk2(lo16(b) * SCALE, hi16(b) * SCALE);
    };
#define QK_LDS(u) do { v4u kf[4]; _Pragma("unroll") for (int s = 0; s < 4; ++s) kf[s] = *LDSP(v4u, ka[s] + ((u) & 7) * 4096); qk(u, kf); } while (0)
#define STEP_K(u) do { if ((u) + LEAD >= 16) { LANDED(u); ISSUE((u) + LEAD); } QK_LDS(u); } while (0)
#pragma unroll
    for (int u = 8; u < 16 - LEAD; ++u) QK_LDS(u);
#pragma unroll
    for (int u = 0; u < 8; ++u) qk(u, kr[u]);
    if (LEAD >= 7) STEP_K(9);
    if (LEAD >= 6) STEP_K(10);
    if (LEAD >= 5) STEP_K(11);
    if (LEAD >= 4) STEP_K(12);
    if (LEAD >= 3) STEP_K(13);
    if (LEAD >= 2) STEP_K(14);
    STEP_K(15);
    STS(3);
    {
      const float m = rbf(rbf(xrow_max(mx)) * SCALE), nm = -m * LOG2E;
      float l0 = 0.f, l1 = 0.f;
#pragma unroll
      for (int u = 0; u < 16; ++u)
#pragma unroll
        for (int j = 0; j < 2; ++j) {
          const float p0 = __builtin_amdgcn_exp2f(fmaf(lo16(sc[u][j]), LOG2E, nm)), p1 = __builtin_amdgcn_exp2f(fmaf(hi16(sc[u][j]), LOG2E, nm));
          l0 += p0; l1 += p1; sc[u][j] = pk2(p0, p1);
        }
      const float l = xrow_sum(l0 + l1);
      if (lane < 8) *LDSP(v2f, L_ML + wave * 64 + lane * 8) = v2f{m, l};
      lds_arrive(L, C_ML, lane);
    }
    STS(4);
    v4f acc[8];
#pragma unroll
    for (int dt = 0; dt < 8; ++dt) acc[dt] = v4f{0.f, 0.f, 0.f, 0.f};
#define STEP_V(k) do { v4s t1[8], t2[8]; \
      LANDED(16 + 2 * (k)); ISSUE(16 + 2 * (k) + LEAD); if (16 + 2 * (k) + LEAD == 31) lds_arrive(L, C_LAST, lane); \
      _Pragma("unroll") for (int dt = 0; dt < 8; ++dt) t1[dt] = __builtin_amdgcn_ds_read_tr16_b64_v4i16(LDSP(v4s, va[dt] + ((2 * (k)) & 7) * 4096)); \
      LANDED(17 + 2 * (k)); ISSUE(17 + 2 * (k) + LEAD); if (17 + 2 * (k) + LEAD == 31) lds_arrive(L, C_LAST, lane); \
      _Pragma("unroll") for (int dt = 0; dt < 8; ++dt) t2[dt] = __builtin_amdgcn_ds_read_tr16_b64_v4i16(LDSP(v4s, va[dt] + ((2 * (k) + 1) & 7) * 4096)); \
      const v8bf pf = __builtin_bit_cast(v8bf, v4u{sc[2 * (k)][0], sc[2 * (k)][1], sc[2 * (k) + 1][0], sc[2 * (k) + 1][1]}); \
      _Pragma("unroll") for (int dt = 0; dt < 8; ++dt) \
        acc[dt] = __builtin_amdgcn_mfma_f32_16x16x32_bf16(__builtin_bit_cast(v8bf, __builtin_shufflevector(t1[dt], t2[dt], 0, 1, 2, 3, 4, 5, 6, 7)), pf, acc[dt], 0, 0, 0); \
    } while (0)
    STEP_V(0); STEP_V(1); STEP_V(2); STEP_V(3); STEP_V(4); STEP_V(5); STEP_V(6);
    lds_wait(L, C_FAC, 1);
    const float fac = *LDSP(float, L_FAC + wave * 32 + (lc & 7) * 4);
    STS(5);
    STEP_V(7);
    STS(6);
    unsigned hm = (lc & 8) ? 0xffffffffu : 0u;
    asm volatile("" : "+v"(hm));
#pragma unroll
    for (int j = 0; j < 4; ++j) {
      v4f o;
#pragma unroll
      for (int r = 0; r < 4; ++r) o[r] = asf((asu(acc[j][r]) & ~hm) | (asu(acc[4 + j][r]) & hm)) * fac;
      *LDSP(v4f, wb + (lc & 7) * 528 + (j + ((lc >> 3) << 2)) * 64 + lq * 16) = o;
    }
    BAR();
    {
      const unsigned ro = (tid >> 5) * 528 + (tid & 31) * 16;
      const v4f o = (*LDSP(v4f, ro) + *LDSP(v4f, 32768 + ro)) + (*LDSP(v4f, 65536 + ro) + *LDSP(v4f, 98304 + ro));
      g_store(ws + WS_GA, (cu * 256 + tid) * 16, o[0], o[1], o[2], o[3], tag);
    }
    STS(7);
    __builtin_amdgcn_s_sleep(DLYA);
    {
      const v4u a = g_wait1(ws + WS_GA, (tid * 256 + px) * 16, tag);
      const float o = wave_sum4(ug(a.x), ug(a.y), ug(a.z), ug(a.w));
      if (lc == 0) *LDSP(u16, L_O16 + wave * 8 + lq * 2) = (u16)pk2(o, o);
    }
    STS(8);
    BAR();
    BAR();
    BAR();
  } else {
    __builtin_amdgcn_s_setprio(2);
    const int aw = wave - 4, ta = tid - 256;
    u64 e0;
    asm volatile("global_load_dwordx2 %0, %1, %2 sc1" : "=&v"(e0) : "v"(0u), "s"(ws + WS_E) : "memory");
    const int rb = px >> 6, cb = px & 63;
    const int R0 = 256 * rb + 64 * aw, row8 = R0 + 8 * (lane >> 3);
    const v4u cr = *(const v4u*)(cvec + row8);
    v4u wm[4], wqr[8];
#pragma unroll
    for (int k = 0; k < 4; ++k) wm[k] = ldnt<v4u>(wmod + (size_t)(R0 + 4 * (lane >> 2) + k) * 6144 + 32 * cb + 8 * (lane & 3));
    const v4u xa = *(const v4u*)(x + 16 * lane), xb = *(const v4u*)(x + 16 * lane + 8);
    const unsigned xr = *(const unsigned*)(x + R0 + 2 * (lane & 31));
#pragma unroll
    for (int i = 0; i < 8; ++i) wqr[i] = ldnt<v4u>(wq + (size_t)(row8 + i) * 4096 + 64 * cb + 8 * (lane & 7));
    BAR();
    asm volatile("s_waitcnt vmcnt(16)" : "+v"(e0)::"memory");
    const u64 e1 = e0 * 6364136223846793005ull + 1442695040888963407ull;
    tag = Tag{(unsigned)(e1 >> 24) & 0x3ffu, (unsigned)(e1 >> 34) & 0x3ffu, (unsigned)(e1 >> 44) & 0x3ffu, (unsigned)(e1 >> 54) & 0x3ffu};
    const Tag2 tag2{(unsigned)(e1 >> 32), (unsigned)e1};
    float sv[8];
#pragma unroll
    for (int k = 0; k < 8; ++k) { const float c = (k & 1) ? hi16(cr[k >> 1]) : lo16(cr[k >> 1]); sv[k] = rbf(c * __builtin_amdgcn_rcpf(1.f + __builtin_amdgcn_exp2f(-LOG2E * c))); }
    {
      unsigned hm = (lane & 4) ? 0xffffffffu : 0u;
      asm volatile("" : "+v"(hm));
      float a[8] = {0, 0, 0, 0, 0, 0, 0, 0};
#pragma unroll
      for (int k = 0; k < 4; ++k) {
        const float s = asf((asu(sv[k]) & ~hm) | (asu(sv[4 + k]) & hm));
#pragma unroll
        for (int j = 0; j < 4; ++j) { a[2 * j] = fmaf(s, lo16(wm[k][j]), a[2 * j]); a[2 * j + 1] = fmaf(s, hi16(wm[k][j]), a[2 * j + 1]); }
      }
      float c0 = tr16(tr32(a[0], a[4]), tr32(a[2], a[6])), c1 = tr16(tr32(a[1], a[5]), tr32(a[3], a[7]));
      c0 += dpp<ROR8>(c0); c1 += dpp<ROR8>(c1);
      const float d0 = c0 + dpp<ROR4>(c0), d1 = c1 + dpp<ROR4>(c1);
      if (!(lane & 8)) *LDSP(float, L_P2 + aw * 128 + ((lane & 3) * 8 + (lane >> 4) * 2 + ((lane >> 2) & 1)) * 4) = (lane & 4) ? d1 : d0;
      STS(1);
      if (lds_arrive(L, C_P2, lane) == 3 && lane < 16) {
        const v2f s = (*LDSP(v2f, L_P2 + lane * 8) + *LDSP(v2f, L_P2 + 128 + lane * 8)) + (*LDSP(v2f, L_P2 + 256 + lane * 8) + *LDSP(v2f, L_P2 + 384 + lane * 8));
        g2_store(ws + WS_G1, (rb * 1024 + 16 * cb + lane) * 16, s[0], s[1], tag2);
      }
    }
    float h0, h1;
    {
      float s1 = 0.f;
#pragma unroll
      for (int j = 0; j < 4; ++j) s1 += (lo16(xa[j]) + hi16(xa[j])) + (lo16(xb[j]) + hi16(xb[j]));
      s1 = wave_sum(s1);
      const float mean = s1 * (1.f / 1024.f);
      float s2 = 0.f;
#pragma unroll
      for (int j = 0; j < 4; ++j) { float d = lo16(xa[j]) - mean; s2 = fmaf(d, d, s2); d = hi16(xa[j]) - mean; s2 = fmaf(d, d, s2);
                                    d = lo16(xb[j]) - mean; s2 = fmaf(d, d, s2); d = hi16(xb[j]) - mean; s2 = fmaf(d, d, s2); }
      s2 = wave_sum(s2);
      const float mb = rbf(mean), rs = __builtin_amdgcn_rcpf(rbf(__builtin_amdgcn_sqrtf(rbf(rbf(s2 * (1.f / 1024.f)) + 1e-5f))));
      h0 = rbf(rbf(lo16(xr) - mb) * rs); h1 = rbf(rbf(hi16(xr) - mb) * rs);
    }
    __builtin_amdgcn_s_sleep(DLY1);
    {
      v4u p[4];
      const unsigned o = ((lane >> 5) * 512 + R0 / 2 + (lane & 31)) * 16;
      for (int s = 0; s < SPIN_G; ++s) { ld4(ws + WS_G1, o, 16384, p); if (__all(g2_ok(p[0], tag2) && g2_ok(p[1], tag2) && g2_ok(p[2], tag2) && g2_ok(p[3], tag2))) break; }
      const float m0 = rbf((asf(p[0].x) + asf(p[1].x)) + (asf(p[2].x) + asf(p[3].x))), m1 = rbf((asf(p[0].z) + asf(p[1].z)) + (asf(p[2].z) + asf(p[3].z)));
      const auto r0 = __builtin_amdgcn_permlane32_swap(asu(m0), asu(m0), false, false), r1 = __builtin_amdgcn_permlane32_swap(asu(m1), asu(m1), false, false);
      h0 = rbf(rbf(h0 * rbf(1.f + asf(r0[1]))) + asf(r0[0])); h1 = rbf(rbf(h1 * rbf(1.f + asf(r1[1]))) + asf(r1[0]));
      if (lane < 32) *LDSP(v2f, L_H + aw * 256 + lane * 8) = v2f{h0, h1};
    }
    STS(2);
    {
      const v4f ha = *LDSP(v4f, L_H + aw * 256 + (lane >> 3) * 32), hb = *LDSP(v4f, L_H + aw * 256 + (lane >> 3) * 32 + 16);
      const float hv[8] = {ha[0], ha[1], ha[2], ha[3], hb[0], hb[1], hb[2], hb[3]};
      gemv64(wqr, hv, lane, LDSP(float, L_P3 + aw * 256));
    }
    if (lds_arrive(L, C_P3, lane) == 3) {
      if (lane < 32) {
        const v2f s = (*LDSP(v2f, L_P3 + lane * 8) + *LDSP(v2f, L_P3 + 256 + lane * 8)) + (*LDSP(v2f, L_P3 + 512 + lane * 8) + *LDSP(v2f, L_P3 + 768 + lane * 8));
        g2_store(ws + WS_G2, (rb * 2048 + 32 * cb + lane) * 16, s[0], s[1], tag2);
      }
    }
    STS(3);
    __builtin_amdgcn_s_sleep(DLY2);
    {
      v4u p[4], q[4];
      const unsigned o = (g * 512 + ta) * 16;
      for (int s = 0; s < SPIN_G; ++s) {
        ld8(ws + WS_G2, o, 32768, p, q);
        if (__all(g2_ok(p[0], tag2) && g2_ok(p[1], tag2) && g2_ok(p[2], tag2) && g2_ok(p[3], tag2) && g2_ok(q[0], tag2) && g2_ok(q[1], tag2) && g2_ok(q[2], tag2) && g2_ok(q[3], tag2))) break;
      }
      *LDSP(unsigned, L_Q + ta * 4) = pk2((asf(p[0].x) + asf(p[1].x)) + (asf(p[2].x) + asf(p[3].x)), (asf(p[0].z) + asf(p[1].z)) + (asf(p[2].z) + asf(p[3].z)));
      *LDSP(unsigned, L_Q + 1024 + ta * 4) = pk2((asf(q[0].x) + asf(q[1].x)) + (asf(q[2].x) + asf(q[3].x)), (asf(q[0].z) + asf(q[1].z)) + (asf(q[2].z) + asf(q[3].z)));
      if (ta == 0) *LDSP(v4u, L_TAG) = v4u{tag.a, tag.b, tag.c, tag.d};
      lds_arrive(L, C_Q, lane);
    }
    STS(4);
    {
      v4u wr[8];
#pragma unroll
      for (int i = 0; i < 8; ++i) wr[i] = ldnt<v4u>(wmod + (size_t)(row8 + i) * 6144 + 2048 + 64 * cb + 8 * (lane & 7));
      v4u pr[6];
#pragma unroll
      for (int k = 0; k < 2; ++k) { const int ch = min(ta + 256 * k, 447);
        pr[k] = *(const v4u*)(w1 + 8 * ch); pr[2 + k] = *(const v4u*)(b1 + 8 * ch); pr[4 + k] = *(const v4u*)(w2 + 8 * ch); }
      gemv64(wr, sv, lane, LDSP(float, L_P4 + aw * 256));
      if (lds_arrive(L, C_P4, lane) == 3 && lane < 16) {
        const v4f s = (*LDSP(v4f, L_P4 + lane * 16) + *LDSP(v4f, L_P4 + 256 + lane * 16)) + (*LDSP(v4f, L_P4 + 512 + lane * 16) + *LDSP(v4f, L_P4 + 768 + lane * 16));
        g_store(ws + WS_G3, (rb * 1024 + 16 * cb + lane) * 16, s[0], s[1], s[2], s[3], tag);
      }
#pragma unroll
      for (int k = 0; k < 2; ++k) { const int ch = min(ta + 256 * k, 447);
        *LDSP(v4u, L_PRM + ch * 16) = pr[k]; *LDSP(v4u, L_PRM + 7168 + ch * 16) = pr[2 + k]; *LDSP(v4u, L_PRM + 14336 + ch * 16) = pr[4 + k]; }
    }
    STS(5);
    lds_wait(L, C_ML, 4);
    STS(6);
    if (aw == 0) {
      const int h = 2 * (lane & 3);
      float mw[4][2], lw[4][2];
#pragma unroll
      for (int w = 0; w < 4; ++w) { const v4f v = *LDSP(v4f, L_ML + w * 64 + h * 8); mw[w][0] = v[0]; lw[w][0] = v[1]; mw[w][1] = v[2]; lw[w][1] = v[3]; }
      float M[2], S[2];
#pragma unroll
      for (int j = 0; j < 2; ++j) {
        M[j] = fmaxf(fmaxf(mw[0][j], mw[1][j]), fmaxf(mw[2][j], mw[3][j])); S[j] = 0.f;
#pragma unroll
        for (int w = 0; w < 4; ++w) S[j] = fmaf(lw[w][j], __builtin_amdgcn_exp2f((mw[w][j] - M[j]) * LOG2E), S[j]);
      }
      if (lane < 4) g_store(ws + WS_GM, (cu * 4 + lane) * 16, M[0], S[0], M[1], S[1], tag);
      __builtin_amdgcn_s_sleep(100);
      v4u r[4];
      const unsigned o = (g * 256 + lane) * 16;
      g_wait4(ws + WS_GM, o, o + 1024, o + 2048, o + 3072, tag, r[0], r[1], r[2], r[3]);
#pragma unroll
      for (int j = 0; j < 2; ++j) {
        float m = fmaxf(fmaxf(ug(r[0][2 * j]), ug(r[1][2 * j])), fmaxf(ug(r[2][2 * j]), ug(r[3][2 * j])));
        m = fmaxf(m, dpp<ROR8>(m)); m = fmaxf(m, dpp<ROR4>(m)); m = xrow_max(m);
        float s = 0.f;
#pragma unroll
        for (int k = 0; k < 4; ++k) s = fmaf(ug(r[k][2 * j + 1]), __builtin_amdgcn_exp2f((ug(r[k][2 * j]) - m) * LOG2E), s);
        s += dpp<ROR8>(s); s += dpp<ROR4>(s); s = xrow_sum(s);
        const float inv = 1.f / s;
        if (lane < 4) {
#pragma unroll
          for (int w = 0; w < 4; ++w) *LDSP(float, L_FAC + w * 32 + (h + j) * 4) = __builtin_amdgcn_exp2f((mw[w][j] - m) * LOG2E) * inv;
        }
      }
      lds_arrive(L, C_FAC, lane);
    }
    float c1[4], sb[4], gaown;
    {
      v4u p[4], q[4], u[4], v[4];
      for (int s = 0; s < SPIN_G; ++s) {
        ld8(ws + WS_G3, (256 + ta) * 16, 16384, p, q);
        if (__all(g_ok(p[0], tag) && g_ok(p[1], tag) && g_ok(p[2], tag) && g_ok(p[3], tag) && g_ok(q[0], tag) && g_ok(q[1], tag) && g_ok(q[2], tag) && g_ok(q[3], tag))) break;
        __builtin_amdgcn_s_sleep(16);
      }
#pragma unroll
      for (int k = 0; k < 4; ++k) {
        sb[k] = rbf((ug(p[0][k]) + ug(p[1][k])) + (ug(p[2][k]) + ug(p[3][k])));
        c1[k] = rbf(1.f + rbf((ug(q[0][k]) + ug(q[1][k])) + (ug(q[2][k]) + ug(q[3][k]))));
      }
      g_wait4(ws + WS_G3, px * 16, 16384 + px * 16, 32768 + px * 16, 49152 + px * 16, tag, u[0], u[1], u[2], u[3]);
      g_wait4(ws + WS_G3, 12288 + px * 16, 28672 + px * 16, 45056 + px * 16, 61440 + px * 16, tag, v[0], v[1], v[2], v[3]);
      const int c = lane & 3;
      float ga4[4], gb4[4];
#pragma unroll
      for (int k = 0; k < 4; ++k) { ga4[k] = (ug(u[0][k]) + ug(u[1][k])) + (ug(u[2][k]) + ug(u[3][k])); gb4[k] = (ug(v[0][k]) + ug(v[1][k])) + (ug(v[2][k]) + ug(v[3][k])); }
      gaown = rbf(c == 0 ? ga4[0] : c == 1 ? ga4[1] : c == 2 ? ga4[2] : ga4[3]);
      gbown = rbf(c == 0 ? gb4[0] : c == 1 ? gb4[1] : c == 2 ? gb4[2] : gb4[3]);
      x1own = lo16(x[4 * px + c]);
      b2v = lo16(b2[0]);
    }
    STS(7);
    lds_wait(L, C_LAST, 4);
    v2u wor[16];
#pragma unroll
    for (int r = 0; r < 16; ++r) wor[r] = ldnt<v2u>(wo + (size_t)((r >> 2) * 1024 + 4 * px + (r & 3)) * 1024 + 4 * ta);
    BAR();
    unsigned wp[8][4];
#pragma unroll
    for (int p = 0; p < 8; ++p) {
      asm volatile("" : "+v"(wor[2 * p]), "+v"(wor[2 * p + 1]));
      wp[p][0] = __builtin_amdgcn_perm(wor[2 * p + 1].x, wor[2 * p].x, 0x05040100u); wp[p][1] = __builtin_amdgcn_perm(wor[2 * p + 1].x, wor[2 * p].x, 0x07060302u);
      wp[p][2] = __builtin_amdgcn_perm(wor[2 * p + 1].y, wor[2 * p].y, 0x05040100u); wp[p][3] = __builtin_amdgcn_perm(wor[2 * p + 1].y, wor[2 * p].y, 0x07060302u);
      asm volatile("" : "+v"(wp[p][0]), "+v"(wp[p][1]), "+v"(wp[p][2]), "+v"(wp[p][3]));
    }
    STS(8);
    BAR();
    {
      const v4u oa = *LDSP(v4u, L_O16), ob = *LDSP(v4u, L_O16 + 16);
      float a[4] = {0.f, 0.f, 0.f, 0.f};
#pragma unroll
      for (int p = 0; p < 8; ++p)
#pragma unroll
        for (int c = 0; c < 4; ++c)
          a[c] = __builtin_amdgcn_fdot2_f32_bf16(__builtin_bit_cast(v2bf, wp[p][c]), __builtin_bit_cast(v2bf, p < 4 ? oa[p] : ob[p - 4]), a[c], false);
      g_store(ws + WS_GB, (cu * 256 + ta) * 16, a[0], a[1], a[2], a[3], tag);
    }
    STS(9);
    __builtin_amdgcn_s_sleep(DLYB);
    {
      const v4u a = g_wait1(ws + WS_GB, (ta * 256 + px) * 16, tag);
      const float s = wave_sum4(ug(a.x), ug(a.y), ug(a.z), ug(a.w));
      if ((lane & 15) == 0) *LDSP(float, L_RB + (lane >> 4) * 16 + aw * 4) = s;
    }
    STS(10);
    BAR();
    if (aw == 0) {
      const v4f s = *LDSP(v4f, L_RB + (lane & 3) * 16);
      x1own = rbf(x1own + rbf(gaown * rbf((s[0] + s[1]) + (s[2] + s[3]))));
      const float a0 = dpp<0x00>(x1own), a1 = dpp<0x55>(x1own), a2 = dpp<0xAA>(x1own), a3 = dpp<0xFF>(x1own);
      if (lane == 0) g_store(ws + WS_GC, px * 16, a0, a1, a2, a3, tag);
    }
    STS(11);
    __builtin_amdgcn_s_sleep(DLYC);
    float h2[4];
    {
      const v4u a = g_wait1(ws + WS_GC, ta * 16, tag);
      if (cu == 0 && ta == 0) asm volatile("global_store_dwordx2 %0, %1, %2 sc1" ::"v"(0u), "v"(e1), "s"(ws + WS_E) : "memory");
      STS(12);
#pragma unroll
      for (int k = 0; k < 4; ++k) h2[k] = ug(a[k]);
      const float u = tr32((h2[0] + h2[1]) + (h2[2] + h2[3]), fmaf(h2[0], h2[0], h2[1] * h2[1]) + fmaf(h2[2], h2[2], h2[3] * h2[3]));
      const float s = row_sum(tr16(u, u));
      if ((lane & 31) == 0) *LDSP(float, L_R1 + (lane >> 5) * 16 + aw * 4) = s;
    }
    BAR();
    {
      const v4f p = *LDSP(v4f, L_R1), q = *LDSP(v4f, L_R1 + 16);
      float mb, rs;
      ln_stats((p[0] + p[1]) + (p[2] + p[3]), (q[0] + q[1]) + (q[2] + q[3]), mb, rs);
#pragma unroll
      for (int k = 0; k < 4; ++k) h2[k] = rbf(rbf(rbf(rbf(h2[k] - mb) * rs) * c1[k]) + sb[k]);
      const float u = tr32((h2[0] + h2[1]) + (h2[2] + h2[3]), fmaf(h2[0], h2[0], h2[1] * h2[1]) + fmaf(h2[2], h2[2], h2[3] * h2[3]));
      const float s = row_sum(tr16(u, u));
      if ((lane & 31) == 0) *LDSP(float, L_R2 + (lane >> 5) * 16 + aw * 4) = s;
      if (ta == px) *LDSP(v4f, L_H2) = v4f{h2[0], h2[1], h2[2], h2[3]};
    }
  }
  float pw1[7], pb1[7], pw2[7];
#pragma unroll
  for (int k = 0; k < 7; ++k) {
    pw1[k] = lo16(*LDSP(u16, L_PRM + (tid + 512 * k) * 2)); pb1[k] = lo16(*LDSP(u16, L_PRM + 7168 + (tid + 512 * k) * 2));
    pw2[k] = lo16(*LDSP(u16, L_PRM + 14336 + (tid + 512 * k) * 2));
  }
  BAR();
  {
    const v4f p = *LDSP(v4f, L_R2), q = *LDSP(v4f, L_R2 + 16), h = *LDSP(v4f, L_H2);
    float mb, rs;
    ln_stats((p[0] + p[1]) + (p[2] + p[3]), (q[0] + q[1]) + (q[2] + q[3]), mb, rs);
#pragma unroll
    for (int k = 0; k < 4; ++k) T[k] = rbf(rbf(h[k] - mb) * rs);
  }
  STS(13);
  {
    float y[4] = {0.f, 0.f, 0.f, 0.f};
    constexpr float G0 = -2.f * 0.7978845608028654f * LOG2E, G1 = G0 * 0.044715f;
#pragma unroll
    for (int k = 0; k < 7; ++k)
#pragma unroll
      for (int c = 0; c < 4; c += 2) {
        const unsigned a = pk2(T[c] * pw1[k], T[c + 1] * pw1[k]);
        const unsigned u = pk2(lo16(a) + pb1[k], hi16(a) + pb1[k]);
        const float u0 = lo16(u), u1 = hi16(u);
        const float e0 = __builtin_amdgcn_exp2f(u0 * fmaf(u0 * u0, G1, G0)), e1 = __builtin_amdgcn_exp2f(u1 * fmaf(u1 * u1, G1, G0));
        const unsigned gl = pk2(u0 * __builtin_amdgcn_rcpf(1.f + e0), u1 * __builtin_amdgcn_rcpf(1.f + e1));
        y[c] = fmaf(lo16(gl), pw2[k], y[c]); y[c + 1] = fmaf(hi16(gl), pw2[k], y[c + 1]);
      }
    const float s = wave_sum4(y[0], y[1], y[2], y[3]);
    if ((lane & 15) == 0) *LDSP(float, L_R3 + (lane >> 4) * 32 + wave * 4) = s;
  }
  STS(14);
  BAR();
  if (wave == 4 && lane < 4) {
    const v4f p = *LDSP(v4f, L_R3 + lane * 32), q = *LDSP(v4f, L_R3 + lane * 32 + 16);
    const float y = rbf(rbf(((p[0] + p[1]) + (p[2] + p[3])) + ((q[0] + q[1]) + (q[2] + q[3]))) + b2v);
    const float o = x1own + rbf(gbown * y);
    out[4 * px + lane] = (u16)pk2(o, o);
  }
  STS(15);
  LGKM0();
  ((unsigned*)(ws + WS_ST))[cu * 512 + tid] = *LDSP(unsigned, L_STAMP + tid * 4);
}
__global__ void pre_kernel(unsigned* hs) { if (threadIdx.x == 0) hs[256 * 512] = (unsigned)__builtin_amdgcn_s_memrealtime(); }
__global__ void __launch_bounds__(256) probe_kernel(const unsigned* hs) {
  const unsigned now = (unsigned)__builtin_amdgcn_s_memrealtime();
  __shared__ unsigned sh[4];
  const int t = threadIdx.x;
  if (t == 0) { sh[0] = ~0u; sh[1] = ~0u; sh[2] = 0; sh[3] = 0; }
  __syncthreads();
  const unsigned tb = hs[0] - 100000u;
  unsigned t0 = ~0u;
  for (int w = 0; w < 8; ++w) t0 = min(t0, hs[(t * 8 + w) * 64] - tb);
  atomicMin(&sh[0], t0); __syncthreads();
  t0 = sh[0];
  unsigned mn = ~0u, mx = 0, sum = 0;
  for (int w = 0 * 4; w < 0 * 4 + 4; ++w) { unsigned v = hs[(t * 8 + w) * 64 + 0] - tb - t0; mn = min(mn, v); mx = max(mx, v); sum += v; }
  atomicMin(&sh[1], mn); atomicMax(&sh[2], mx); atomicAdd(&sh[3], sum); __syncthreads();
  if (t == 0) {
    mn = sh[1]; mx = sh[2]; sum = sh[3];
    unsigned val = mn;
    if (val > 20000) val = 20000;
    const unsigned a = (unsigned)__builtin_amdgcn_s_memrealtime();
    while ((unsigned)__builtin_amdgcn_s_memrealtime() - a < val * 100) __builtin_amdgcn_s_sleep(8);
  }
}
}

extern "C" void kernel_launch(void* const* d_in, const int* in_sizes, int n_in, void* d_out, int out_size, void* d_ws, size_t ws_size, hipStream_t stream) {
  (void)in_sizes; (void)n_in; (void)out_size; (void)ws_size;
  (void)hipFuncSetAttribute((const void*)block_kernel, hipFuncAttributeMaxDynamicSharedMemorySize, L_TOTAL);
  hipLaunchKernelGGL(block_kernel, dim3(NCU), dim3(NTHR), L_TOTAL, stream, (const u16*)d_in[0], (const unsigned char*)d_in[1], (const unsigned char*)d_in[2],
                     (const u16*)d_in[3], (const u16*)d_in[4], (const u16*)d_in[6], (const u16*)d_in[7], (const u16*)d_in[8], (const u16*)d_in[9],
                     (const u16*)d_in[10], (const u16*)d_in[11], (u16*)d_out, (unsigned char*)d_ws);
  hipLaunchKernelGGL(probe_kernel, dim3(1), dim3(256), 0, stream, (const unsigned*)((char*)d_ws + WS_ST));
}
```

```cpp
#include <hip/hip_runtime.h>
#include <hip/hip_bf16.h>
#include <stdint.h>

#define PACE_0 310
#define PACE_K 600
#define PACE_V 580
#define STAG 15
#define WCNT 6
#define T_1 500
#define T_2 1000
#define FIT1 70
#define FIT 40
#define WO_0 25
#define WO_D 28
#define DLYA 10
#define DLYB 16
#define DLYC 19
#define CODE_BYTES 49028

namespace {
#define LAS __attribute__((address_space(3)))
typedef unsigned short u16;
typedef unsigned long long u64;
typedef unsigned v4u __attribute__((ext_vector_type(4)));
typedef unsigned v2u __attribute__((ext_vector_type(2)));
typedef float v4f __attribute__((ext_vector_type(4)));
typedef float v2f __attribute__((ext_vector_type(2)));
typedef short v4s __attribute__((ext_vector_type(4)));
typedef __bf16 v8bf __attribute__((ext_vector_type(8)));
typedef __bf16 v2bf __attribute__((ext_vector_type(2)));

constexpr int NCU = 256, NTHR = 512;
constexpr unsigned WS_E = 0, WS_GS = 2048, WS_GC = 4096, WS_GD = 8192, WS_GM = 16384, WS_GT = 32768, WS_G1 = 65536, WS_G3 = WS_G1 + 65536, WS_G2 = WS_G3 + 65536;
constexpr unsigned WS_GA = 1u << 20, WS_GB = 2u << 20;
constexpr unsigned RING = 9 * 4096;
constexpr unsigned L_TAB = 3 * 4096;
constexpr unsigned L_Q = 4 * RING, L_P2 = L_Q + 2048, L_P3 = L_P2 + 1024, L_P4 = L_P3 + 1024, L_H = L_P4 + 1024, L_ML = L_H + 1024, L_W = L_ML + 256,
                   L_O16 = L_W + 1024, L_RB = L_O16 + 64, L_R1 = L_RB + 64, L_R2 = L_R1 + 32, L_H2 = L_R2 + 32, L_R3 = L_H2 + 16, L_TAG = L_R3 + 384,
                   L_TB = L_TAG + 16, L_CTL = L_TB + 16, L_PF = L_CTL + 64, L_END = L_PF + 128;
enum { C_P2, C_P3, C_P4, C_PA, C_PB, C_PC, C_Q, C_ML, C_MID, C_LAST, C_TAB, C_W, C_O, C_TB, C_PP, C_PD };
constexpr int SPIN_G = 1 << 17, SPIN_L = 1 << 22;
constexpr float LOG2E = 1.4426950408889634f, SCALE = 0.08838834764831845f;

struct Tag { unsigned a, b, c, d; };

__device__ __forceinline__ float asf(unsigned u) { return __builtin_bit_cast(float, u); }
__device__ __forceinline__ unsigned asu(float f) { return __builtin_bit_cast(unsigned, f); }
__device__ __forceinline__ unsigned pk2(float lo, float hi) { v2f p = {lo, hi}; return __builtin_bit_cast(unsigned, __builtin_convertvector(p, v2bf)); }
__device__ __forceinline__ float lo16(unsigned u) { return asf(u << 16); }
__device__ __forceinline__ float hi16(unsigned u) { return asf(u & 0xffff0000u); }
__device__ __forceinline__ float rbf(float f) { return lo16(pk2(f, f)); }
template <int C> __device__ __forceinline__ float dpp(float x) { return asf((unsigned)__builtin_amdgcn_mov_dpp((int)asu(x), C, 0xf, 0xf, true)); }
constexpr int XOR1 = 0xB1, XOR2 = 0x4E, ROR8 = 0x128, ROR4 = 0x124, ROR2 = 0x122, ROR1 = 0x121;
__device__ __forceinline__ float tr32(float a, float b) { auto r = __builtin_amdgcn_permlane32_swap(asu(a), asu(b), false, false); return asf(r[0]) + asf(r[1]); }
__device__ __forceinline__ float tr16(float a, float b) { auto r = __builtin_amdgcn_permlane16_swap(asu(a), asu(b), false, false); return asf(r[0]) + asf(r[1]); }
__device__ __forceinline__ float row_sum(float x) { x += dpp<ROR8>(x); x += dpp<ROR4>(x); x += dpp<ROR2>(x); x += dpp<ROR1>(x); return x; }
__device__ __forceinline__ float wave_sum(float x) { return row_sum(tr16(tr32(x, x), tr32(x, x))); }
__device__ __forceinline__ float wave_sum4(float a, float b, float c, float d) { return row_sum(tr16(tr32(a, c), tr32(b, d))); }
__device__ __forceinline__ float xrow_max(float x) {
  auto s = __builtin_amdgcn_permlane16_swap(asu(x), asu(x), false, false); x = fmaxf(asf(s[0]), asf(s[1]));
  auto t = __builtin_amdgcn_permlane32_swap(asu(x), asu(x), false, false); return fmaxf(asf(t[0]), asf(t[1]));
}
__device__ __forceinline__ float xrow_sum(float x) { x = tr16(x, x); return tr32(x, x); }
__device__ __forceinline__ float wave_max(float x) { x = fmaxf(x, dpp<ROR8>(x)); x = fmaxf(x, dpp<ROR4>(x)); x = fmaxf(x, dpp<ROR2>(x)); return xrow_max(fmaxf(x, dpp<ROR1>(x))); }

__device__ __forceinline__ unsigned tg(float v, unsigned t) { return ((asu(v) + 0x200u) & ~0x3ffu) | t; }
__device__ __forceinline__ float ug(unsigned v) { return asf(v & ~0x3ffu); }
__device__ __forceinline__ void g_store(const void* base, unsigned off, float a, float b, float c, float d, const Tag& t) {
  v4u v = {tg(a, t.a), tg(b, t.b), tg(c, t.c), tg(d, t.d)};
  asm volatile("global_store_dwordx4 %0, %1, %2 sc1\n\ts_nop 1" ::"v"(off), "v"(v), "s"(base) : "memory");
}
__device__ __forceinline__ bool g_ok(const v4u& x, const Tag& t) { return (((x.x ^ t.a) | (x.y ^ t.b) | (x.z ^ t.c) | (x.w ^ t.d)) & 0x3ffu) == 0u; }
__device__ __forceinline__ v4u g_wait1(const void* base, unsigned off, const Tag& t) {
  v4u x;
#pragma unroll 1
  for (int s = 0; s < SPIN_G; ++s) {
    asm volatile("global_load_dwordx4 %0, %1, %2 sc1\n\ts_waitcnt vmcnt(0)" : "=&v"(x) : "v"(off), "s"(base) : "memory");
    if (__all(g_ok(x, t))) break;
  }
  return x;
}
template <int NAP = 24> __device__ __forceinline__ void g_retry(const void* base, unsigned off, const Tag& t, v4u& x) {
#pragma unroll 1
  for (int s = 0; s < SPIN_G && !__all(g_ok(x, t)); ++s) {
    if (NAP) __builtin_amdgcn_s_sleep(NAP);
    if (!g_ok(x, t)) asm volatile("global_load_dwordx4 %0, %1, %2 sc1\n\ts_waitcnt vmcnt(0)" : "=&v"(x) : "v"(off), "s"(base) : "memory");
  }
}
__device__ __forceinline__ void gb_store(const void* base, unsigned off, const v4f& v, const Tag& t) {
  const unsigned e = asu(fmaxf(fmaxf(fabsf(v[0]), fabsf(v[1])), fmaxf(fabsf(v[2]), fabsf(v[3])))) >> 23;
  const unsigned tt[4] = {t.a, t.b, t.c, t.d};
  v4u w;
#pragma unroll
  for (int k = 0; k < 4; ++k) {
    const int q = min((int)__builtin_rintf(__builtin_amdgcn_ldexpf(v[k], 149 - (int)e)), (1 << 23) - 1);
    w[k] = ((unsigned)q << 8) | tt[k] | ((e >> (2 * k)) & 3u);
  }
  asm volatile("global_store_dwordx4 %0, %1, %2 sc1\n\ts_nop 1" ::"v"(off), "v"(w), "s"(base) : "memory");
}
__device__ __forceinline__ bool gb_ok(const v4u& x, const Tag& t) { return (((x.x ^ t.a) | (x.y ^ t.b) | (x.z ^ t.c) | (x.w ^ t.d)) & 0xfcu) == 0u; }
__device__ __forceinline__ v4f gb_val(const v4u& x) {
  const unsigned e = (x.x & 3u) | ((x.y & 3u) << 2) | ((x.z & 3u) << 4) | ((x.w & 3u) << 6);
  const float sc = e == 255u ? __builtin_nanf("") : __builtin_amdgcn_ldexpf(1.f, (int)e - 157);
  return v4f{(float)(int)(x.x & ~0xffu), (float)(int)(x.y & ~0xffu), (float)(int)(x.z & ~0xffu), (float)(int)(x.w & ~0xffu)} * sc;
}

#define UNTIL(t) do { while ((int)((unsigned)__builtin_amdgcn_s_memrealtime() - tstart) < (t)) __builtin_amdgcn_s_sleep(1); } while (0)
#define LDSP(T, off) ((LAS T*)(L + (off)))
#define LGKM0() asm volatile("s_waitcnt lgkmcnt(0)" ::: "memory")
#define BAR() do { asm volatile("s_waitcnt lgkmcnt(0)" ::: "memory"); __builtin_amdgcn_s_barrier(); asm volatile("" ::: "memory"); } while (0)
__device__ __forceinline__ unsigned lds_arrive(LAS unsigned char* L, int c, int lane) {
  LGKM0();
  unsigned r = 0;
  if (lane == 0) r = __hip_atomic_fetch_add(LDSP(unsigned, L_CTL) + c, 1u, __ATOMIC_RELAXED, __HIP_MEMORY_SCOPE_WORKGROUP);
  r = __builtin_amdgcn_readfirstlane(r);
  asm volatile("" ::: "memory");
  return r;
}
__device__ __forceinline__ void lds_post(LAS unsigned char* L, int c, int lane) {
  if (lane == 0) (void)__hip_atomic_fetch_add(LDSP(unsigned, L_CTL) + c, 1u, __ATOMIC_RELAXED, __HIP_MEMORY_SCOPE_WORKGROUP);
}
__device__ __forceinline__ void lds_wait(LAS unsigned char* L, int c, unsigned n) {
#pragma unroll 1
  for (int s = 0; s < SPIN_L; ++s) {
    if (__builtin_amdgcn_readfirstlane(*(volatile LAS unsigned*)(LDSP(unsigned, L_CTL) + c)) >= n) break;
    __builtin_amdgcn_s_sleep(1);
  }
  asm volatile("" ::: "memory");
}
__device__ __forceinline__ void lds_spin(LAS unsigned char* L, int c, unsigned n) {
#pragma unroll 1
  for (int s = 0; s < SPIN_L; ++s)
    if (__builtin_amdgcn_readfirstlane(*(volatile LAS unsigned*)(LDSP(unsigned, L_CTL) + c)) >= n) break;
  asm volatile("" ::: "memory");
}
constexpr int LEAD = 3, NREG = 9, MIDU = 26;
constexpr int slot_of(int u) { return u < 16 ? u % 7 : u < 18 ? u - 9 : (u - 16) % 7; }
constexpr int pace(int v) { return PACE_0 + (v < 16 ? v * PACE_K : 16 * PACE_K + (v - 16) * PACE_V) / 10; }
template <int N> __device__ __forceinline__ void wait_vm() { asm volatile("s_waitcnt vmcnt(%0)" ::"n"(N) : "memory"); }
__device__ __forceinline__ void dma4(unsigned lds_dst, const void* src, unsigned o0, unsigned o1, unsigned o2, unsigned o3) {
  unsigned keep;
  asm volatile("s_waitcnt lgkmcnt(0)\n\ts_mov_b32 %0, m0\n\ts_mov_b32 m0, %1\n\ts_nop 0\n\t"
               "global_load_lds_dwordx4 %3, %2 nt\n\tglobal_load_lds_dwordx4 %4, %2 offset:1024 nt\n\t"
               "global_load_lds_dwordx4 %5, %2 offset:2048 nt\n\tglobal_load_lds_dwordx4 %6, %2 offset:3072 nt\n\ts_mov_b32 m0, %0"
               : "=&s"(keep) : "s"(lds_dst), "s"(src), "v"(o0), "v"(o1), "v"(o2), "v"(o3) : "memory");
}
template <class T> __device__ __forceinline__ T ldnt(const void* p) { return __builtin_nontemporal_load((const T*)p); }
__device__ __forceinline__ void ln_stats(float s1, float s2, float a, float& mb, float& rs) {
  const float d = s1 * (1.f / 1024.f), var = fmaxf(s2 * (1.f / 1024.f) - d * d, 0.f);
  mb = rbf(a + d);
  rs = __builtin_amdgcn_rcpf(rbf(__builtin_amdgcn_sqrtf(rbf(rbf(var) + 1e-5f))));
}
__device__ __forceinline__ float moments(const v4f& v, float a) {
  const v4f d = v - a;
  const float u = tr32((d[0] + d[1]) + (d[2] + d[3]), fmaf(d[0], d[0], d[1] * d[1]) + fmaf(d[2], d[2], d[3] * d[3]));
  return row_sum(tr16(u, u));
}
__device__ __forceinline__ float silu(float c) { return rbf(c / (1.f + expf(-c))); }
__device__ __forceinline__ v4f rbf4(const v4f& v) { const unsigned a = pk2(v[0], v[1]), b = pk2(v[2], v[3]); return v4f{lo16(a), hi16(a), lo16(b), hi16(b)}; }
__device__ __forceinline__ void cols64(const float (&acc)[8], int lane, LAS float* dst) {
  const float c0 = tr16(tr32(acc[0], acc[4]), tr32(acc[2], acc[6])), c1 = tr16(tr32(acc[1], acc[5]), tr32(acc[3], acc[7]));
  const float d0 = c0 + dpp<ROR8>(c0), d1 = c1 + dpp<ROR8>(c1);
  dst[(lane & 7) * 8 + (lane >> 3)] = (lane & 8) ? d1 : d0;
}
__device__ __forceinline__ void gemv64(const v4u (&w)[8], const float (&r)[8], int lane, LAS float* dst) {
  float acc[8] = {0, 0, 0, 0, 0, 0, 0, 0};
#pragma unroll
  for (int i = 0; i < 8; ++i)
#pragma unroll
    for (int j = 0; j < 4; ++j) { acc[2 * j] = fmaf(r[i], lo16(w[i][j]), acc[2 * j]); acc[2 * j + 1] = fmaf(r[i], hi16(w[i][j]), acc[2 * j + 1]); }
  cols64(acc, lane, dst);
}
struct Mix { v2f w1[7], b1[7]; unsigned w2[7]; };
__device__ __forceinline__ v2f unp(unsigned p) { return v2f{lo16(p), hi16(p)}; }
__device__ __forceinline__ float dot2(unsigned a, unsigned b, float c) { return __builtin_amdgcn_fdot2_f32_bf16(__builtin_bit_cast(v2bf, a), __builtin_bit_cast(v2bf, b), c, false); }
template <bool PM>
__device__ __forceinline__ float mixer2(const Mix& P, float t0, float t1) {
  constexpr float G0 = -2.f * 0.7978845608028654f * LOG2E, G1 = G0 * 0.044715f;
  float y[2] = {0.f, 0.f};
#pragma unroll
  for (int k = 0; k < 7; ++k) {
    const v2f a0 = P.w1[k] * t0, r0 = unp(pk2(a0[0], a0[1]));
    v2f r1 = -r0;
    if (!PM) { const v2f a1 = P.w1[k] * t1; r1 = unp(pk2(a1[0], a1[1])); }
#pragma unroll
    for (int c = 0; c < 2; ++c) {
      const v2f b = (c ? r1 : r0) + P.b1[k];
      const v2f u = unp(pk2(b[0], b[1]));
      const v2f z = u * __builtin_elementwise_fma(u * u, v2f{G1, G1}, v2f{G0, G0});
      const v2f d = v2f{__builtin_amdgcn_exp2f(z[0]), __builtin_amdgcn_exp2f(z[1])} + 1.f;
      const v2f gl = u * v2f{__builtin_amdgcn_rcpf(d[0]), __builtin_amdgcn_rcpf(d[1])};
      y[c] = dot2(pk2(gl[0], gl[1]), P.w2[k], y[c]);
    }
  }
  const float h = tr32(y[0], y[1]);
  return row_sum(tr16(h, h));
}
constexpr unsigned TAB_LO = 113u << 7, TAB_Z = 2303u;
__device__ __forceinline__ float tab_input(unsigned m) { return m == TAB_Z ? 0.f : asf((TAB_LO + m) << 16); }

typedef unsigned v16u __attribute__((ext_vector_type(16)));
typedef unsigned v8u __attribute__((ext_vector_type(8)));
template <class T> __device__ __forceinline__ T* mkptr(unsigned lo, unsigned hi) { return (T*)(__attribute__((address_space(1))) T*)(((u64)hi << 32) | lo); }

__global__ void __launch_bounds__(NTHR, 2) block_kernel(const u16*, const unsigned char*, const unsigned char*, const u16*, const u16*, const u16*, const u16*,
                                                         const u16*, const u16*, const u16*, const u16*, u16*, unsigned char*) {
  v16u ka0; v8u ka1; v2u ka2;
  asm volatile("s_load_dwordx16 %0, %3, 0x0\n\ts_load_dwordx8 %1, %3, 0x40\n\ts_load_dwordx2 %2, %3, 0x60"
               : "=&s"(ka0), "=&s"(ka1), "=&s"(ka2) : "s"(__builtin_amdgcn_kernarg_segment_ptr()));
  extern __shared__ __attribute__((aligned(16))) unsigned char smem[];
  LAS unsigned char* L = (LAS unsigned char*)smem;
  const int tid = threadIdx.x, lane = tid & 63, wave = __builtin_amdgcn_readfirstlane(tid >> 6);
  const int cu = blockIdx.x, g = cu >> 6, px = (cu & 7) * 32 + (cu >> 3);
  const int rb = px >> 6, cb = px & 63;
  const int cc = (cb & ~7) | ((cb + 3) & 7);
  Tag tag;
  unsigned tstart = (unsigned)__builtin_amdgcn_s_memrealtime();
  if (wave == 4) {
    u64 pc;
    asm volatile("s_getpc_b64 %0" : "=s"(pc));
    const unsigned off = (((cu >> 3) * 16 + lane) & 511) * 128u;
    unsigned keep;
    if (lane < 32 && off < CODE_BYTES)
      asm volatile("s_mov_b32 %0, m0\n\ts_mov_b32 m0, %3\n\ts_nop 0\n\tglobal_load_lds_dword %1, %2\n\ts_mov_b32 m0, %0" : "=&s"(keep) : "v"(off), "s"(pc & ~127ull), "s"(L_PF) : "memory");
  }
  if (tid < 16) LDSP(unsigned, L_CTL)[tid] = 0;
  asm volatile("s_waitcnt lgkmcnt(0)" : "+s"(ka0), "+s"(ka1), "+s"(ka2)::"memory");
  const u16* const x = mkptr<const u16>(ka0[0], ka0[1]);
  const unsigned char* const Kc = mkptr<const unsigned char>(ka0[2], ka0[3]);
  const unsigned char* const Vc = mkptr<const unsigned char>(ka0[4], ka0[5]);
  const u16* const wq = mkptr<const u16>(ka0[6], ka0[7]);
  const u16* const wo = mkptr<const u16>(ka0[8], ka0[9]);
  const u16* const w1 = mkptr<const u16>(ka0[10], ka0[11]);
  const u16* const b1 = mkptr<const u16>(ka0[12], ka0[13]);
  const u16* const w2 = mkptr<const u16>(ka0[14], ka0[15]);
  const u16* const b2 = mkptr<const u16>(ka1[0], ka1[1]);
  const u16* const cvec = mkptr<const u16>(ka1[2], ka1[3]);
  const u16* const wmod = mkptr<const u16>(ka1[4], ka1[5]);
  u16* const out = mkptr<u16>(ka1[6], ka1[7]);
  unsigned char* const ws = mkptr<unsigned char>(ka2[0], ka2[1]);
  const __amdgpu_buffer_rsrc_t wsr = __builtin_amdgcn_make_buffer_rsrc(ws, 0, 1 << 20, 0x00020000);
#define GLD(off) __builtin_amdgcn_raw_buffer_load_b128(wsr, (off), 0, 16)
  const int r8 = px >> 5, c5 = (px & 24) | ((px + 5) & 7);
  const unsigned row4 = 128 * r8 + 32 * (wave & 3) + 4 * (lane >> 3), oob = wave < 4 ? 0x80000000u : 0u;
  const __amdgpu_buffer_rsrc_t wmr = __builtin_amdgcn_make_buffer_rsrc((void*)wmod, 0, 1024 * 6144 * 2, 0x00020000);
  const __amdgpu_buffer_rsrc_t cvr = __builtin_amdgcn_make_buffer_rsrc((void*)cvec, 0, 2048, 0x00020000);
  v4u wm[4];
#pragma unroll
  for (int k = 0; k < 4; ++k) wm[k] = __builtin_amdgcn_raw_buffer_load_b128(wmr, ((row4 + k) * 12288 + 128 * c5 + 16 * (lane & 7)) | oob, 0, 2);
  __builtin_amdgcn_sched_barrier(0);
  const v2u cq = __builtin_amdgcn_raw_buffer_load_b64(cvr, (row4 * 2) | oob, 0, 0);
  unsigned zv = WS_E | oob;
  asm volatile("" : "+v"(zv));
  const v2u er = __builtin_amdgcn_raw_buffer_load_b64(wsr, zv, 0, 16);
  __builtin_amdgcn_sched_barrier(0);
  BAR();
  __builtin_amdgcn_sched_barrier(0);
  if (__builtin_expect(wave < 4, 1)) {
    asm volatile("" ::"v"(wm[0]), "v"(wm[1]), "v"(wm[2]), "v"(wm[3]), "v"(cq), "v"(er));
    const unsigned wb = wave * RING;
    {
      const unsigned char* src = (const unsigned char*)(wq + (size_t)(256 * rb + 64 * wave) * 4096 + 64 * cb);
      const unsigned o = (lane >> 3) * 65536 + (lane & 7) * 16;
      dma4(wb + 5 * 4096, src, o, o + 7168, o + 14336, o + 21504);
      if (wave < 3) {
        const unsigned char* psrc = (const unsigned char*)(wave == 0 ? w1 : wave == 1 ? b1 : w2);
        unsigned keep;
        asm volatile("s_mov_b32 %0, m0\n\ts_mov_b32 m0, %1\n\ts_nop 0\n\t"
                     "global_load_lds_dwordx4 %5, %2\n\tglobal_load_lds_dwordx4 %5, %2 offset:1024\n\tglobal_load_lds_dwordx4 %5, %2 offset:2048\n\t"
                     "global_load_lds_dwordx4 %5, %2 offset:3072\n\ts_mov_b32 m0, %3\n\ts_nop 0\n\t"
                     "global_load_lds_dwordx4 %5, %4\n\tglobal_load_lds_dwordx4 %5, %4 offset:1024\n\tglobal_load_lds_dwordx4 %5, %4 offset:2048\n\t"
                     "s_mov_b32 m0, %0"
                     : "=&s"(keep) : "s"(wb + 3 * 4096), "s"(psrc), "s"(wb + 4 * 4096), "s"(psrc + 4096), "v"(lane * 16) : "memory");
      }
      dma4(wb + 6 * 4096, src + 32768, o, o + 7168, o + 14336, o + 21504);
      wait_vm<4>();
      lds_arrive(L, C_PP, lane);
    }
    {
      const unsigned char* src = (const unsigned char*)(wmod + (size_t)(256 * rb + 64 * wave) * 6144 + 2048 + 64 * cc);
      const unsigned o = (lane >> 3) * 98304 + (lane & 7) * 16;
      dma4(wb + 7 * 4096, src, o, o + 11264, o + 22528, o + 33792);
      dma4(wb + 8 * 4096, src + 49152, o, o + 11264, o + 22528, o + 33792);
    }
    __builtin_amdgcn_s_setprio(1);
    const int stag = ((wave + cu) & 3) * STAG;
    bool based = false;
    const unsigned lq = lane >> 4, lc = lane & 15;
    const unsigned char* kb = Kc + ((size_t)g << 24) + wave * 4096;
    const unsigned char* vb = Vc + ((size_t)g << 24) + wave * 4096;
    const unsigned ok0 = lq * 256 + ((lc ^ lq) << 4), ok1 = ok0 ^ 64, ok2 = ok0 ^ 128, ok3 = ok0 ^ 192;
    const unsigned ov0 = lq * 256 + ((lc ^ (lq << 1)) << 4), ov1 = ov0 ^ 128;
    unsigned ka[4], va[8];
#pragma unroll
    for (int s = 0; s < 4; ++s) ka[s] = wb + lc * 256 + (((4 * s + lq) ^ lc) << 4);
    { const unsigned row = 4 * lq + (lc >> 2);
#pragma unroll
      for (int dt = 0; dt < 8; ++dt) va[dt] = wb + row * 256 + ((dt ^ (row & 7)) << 5) + ((lane & 3) << 3); }
#define UOFF(u) (unsigned)(((((u) + g) & 15) << 20) + (((cu + ((u) & 15) * 9) & 63) << 14))
#define ISSUE(u) do { UNTIL(pace(u) + stag); if ((u) >= 2) wait_vm<WCNT>(); \
                      if ((u) == 3) lds_wait(L, C_PB, 4); \
                      if ((u) == 5) lds_wait(L, C_PD, 4); \
                      if ((u) == 16) lds_wait(L, C_P4, 4); \
                      if ((u) >= 6 && (u) <= 14 && !based && __builtin_amdgcn_readfirstlane(*(volatile LAS unsigned*)(LDSP(unsigned, L_CTL) + C_TB))) { \
                        based = true; asm volatile("" ::: "memory"); tstart = __builtin_amdgcn_readfirstlane(*LDSP(unsigned, L_TB)); } \
                      if ((u) < 16) dma4(wb + slot_of(u) * 4096, kb + UOFF(u), ok0, ok1, ok2, ok3); \
                      else dma4(wb + slot_of(u) * 4096, vb + UOFF(u), ov0, ov1, ov0, ov1); \
                      if ((u) == 31) lds_post(L, C_LAST, lane); } while (0)
#define ADVANCE(u) do { wait_vm<4 * (((u) + LEAD - 1 < 31 ? (u) + LEAD - 1 : 31) - (u))>(); \
                        if ((u) == MIDU) lds_post(L, C_MID, lane); \
                        if ((u) + LEAD <= 31) ISSUE((u) + LEAD); } while (0)
    wait_vm<8>();
    lds_arrive(L, C_PA, lane);
    ISSUE(0); ISSUE(1);
    wait_vm<8>();
    lds_arrive(L, C_PC, lane);
    ISSUE(2);
    v4u kr[NREG][4];
#define STEP_REG(u) do { ADVANCE(u); _Pragma("unroll") for (int s = 0; s < 4; ++s) kr[u][s] = *LDSP(v4u, ka[s] + slot_of(u) * 4096); } while (0)
    STEP_REG(0); STEP_REG(1); STEP_REG(2); STEP_REG(3); STEP_REG(4); STEP_REG(5); STEP_REG(6); STEP_REG(7); STEP_REG(8);
    ADVANCE(9); ADVANCE(10); ADVANCE(11); ADVANCE(12); ADVANCE(13); ADVANCE(14);
    lds_wait(L, C_Q, 4);
    v8bf qf[4];
#pragma unroll
    for (int s = 0; s < 4; ++s) qf[s] = __builtin_bit_cast(v8bf, *LDSP(v4u, L_Q + (lc & 7) * 256 + (4 * s + lq) * 16));
    { const v4u tv = *LDSP(v4u, L_TAG); tag = Tag{tv.x, tv.y, tv.z, tv.w}; }
    unsigned sc[16][2];
    float mx = -INFINITY;
    auto qk2 = [&](int u, const v4u (&ku)[4], int w, const v4u (&kw)[4]) {
      v4f d = {0.f, 0.f, 0.f, 0.f}, e = {0.f, 0.f, 0.f, 0.f};
#pragma unroll
      for (int s = 0; s < 4; ++s) {
        d = __builtin_amdgcn_mfma_f32_16x16x32_bf16(__builtin_bit_cast(v8bf, ku[s]), qf[s], d, 0, 0, 0);
        e = __builtin_amdgcn_mfma_f32_16x16x32_bf16(__builtin_bit_cast(v8bf, kw[s]), qf[s], e, 0, 0, 0);
      }
      mx = fmaxf(fmaxf(fmaxf(mx, fmaxf(d[0], d[1])), fmaxf(d[2], d[3])), fmaxf(fmaxf(e[0], e[1]), fmaxf(e[2], e[3])));
      const v4f ds = rbf4(d) * SCALE, es = rbf4(e) * SCALE;
      sc[u][0] = pk2(ds[0], ds[1]); sc[u][1] = pk2(ds[2], ds[3]); sc[w][0] = pk2(es[0], es[1]); sc[w][1] = pk2(es[2], es[3]);
      asm volatile("" : "+v"(sc[u][0]), "+v"(sc[u][1]), "+v"(sc[w][0]), "+v"(sc[w][1]), "+v"(mx));
    };
#define RD_K(kf, u) v4u kf[4]; _Pragma("unroll") for (int s = 0; s < 4; ++s) kf[s] = *LDSP(v4u, ka[s] + slot_of(u) * 4096)
#define QK_LR(u, r) do { RD_K(kf, u); qk2(r, kr[r], u, kf); } while (0)
    QK_LR(9, 0); QK_LR(10, 1);
    ADVANCE(15); QK_LR(11, 2); QK_LR(12, 3);
    ADVANCE(16); QK_LR(13, 4); QK_LR(14, 5);
    ADVANCE(17); QK_LR(15, 6); qk2(7, kr[7], 8, kr[8]);
    const float m = rbf(rbf(xrow_max(mx)) * SCALE), nm = -m * LOG2E;
    float l0 = 0.f, l1 = 0.f;
#define SOFTMAX(u0, u1) do { _Pragma("unroll") for (int u = (u0); u < (u1); ++u) _Pragma("unroll") for (int j = 0; j < 2; ++j) { \
        const float p0 = __builtin_amdgcn_exp2f(fmaf(lo16(sc[u][j]), LOG2E, nm)), p1 = __builtin_amdgcn_exp2f(fmaf(hi16(sc[u][j]), LOG2E, nm)); \
        l0 += p0; l1 += p1; sc[u][j] = pk2(p0, p1); asm volatile("" : "+v"(sc[u][j]), "+v"(l0), "+v"(l1)); } } while (0)
    v4f acc[4];
#pragma unroll
    for (int dt = 0; dt < 4; ++dt) acc[dt] = v4f{0.f, 0.f, 0.f, 0.f};
    const unsigned hm = (lc & 8) ? 0xffffffffu : 0u;
    v4s t1[8], t2[8];
#define RD_V(t, u) do { _Pragma("unroll") for (int dt = 0; dt < 8; ++dt) t[dt] = __builtin_amdgcn_ds_read_tr16_b64_v4i16(LDSP(v4s, va[dt] + slot_of(u) * 4096)); } while (0)
#define PV(k) do { const v4u pp = v4u{sc[2 * (k)][0], sc[2 * (k)][1], sc[2 * (k) + 1][0], sc[2 * (k) + 1][1]}; \
      const v8bf pa = __builtin_bit_cast(v8bf, pp & ~hm), pb = __builtin_bit_cast(v8bf, pp & hm); \
      _Pragma("unroll") for (int dt = 0; dt < 8; ++dt) \
        acc[dt & 3] = __builtin_amdgcn_mfma_f32_16x16x32_bf16(__builtin_bit_cast(v8bf, __builtin_shufflevector(t1[dt], t2[dt], 0, 1, 2, 3, 4, 5, 6, 7)), dt < 4 ? pa : pb, acc[dt & 3], 0, 0, 0); } while (0)
#define STEP_V(k) do { ADVANCE(16 + 2 * (k)); RD_V(t1, 16 + 2 * (k)); ADVANCE(17 + 2 * (k)); RD_V(t2, 17 + 2 * (k)); PV(k); } while (0)
    ADVANCE(18); RD_V(t1, 16); RD_V(t2, 17); SOFTMAX(0, 6); PV(0);
    ADVANCE(19); RD_V(t1, 18); RD_V(t2, 19); SOFTMAX(6, 11); PV(1);
    ADVANCE(20); RD_V(t1, 20); SOFTMAX(11, 16);
    { const float l = xrow_sum(l0 + l1);
      if (lane < 8) *LDSP(v2f, L_ML + wave * 64 + lane * 8) = v2f{m, l}; }
    if (lds_arrive(L, C_ML, lane) == 3) {
      float mw[4][2], lw[4][2], M[2], S[2];
#pragma unroll
      for (int w = 0; w < 4; ++w) { const v4f v = *LDSP(v4f, L_ML + w * 64 + (lane & 3) * 16); mw[w][0] = v[0]; lw[w][0] = v[1]; mw[w][1] = v[2]; lw[w][1] = v[3]; }
#pragma unroll
      for (int j = 0; j < 2; ++j) {
        M[j] = fmaxf(fmaxf(mw[0][j], mw[1][j]), fmaxf(mw[2][j], mw[3][j])); S[j] = 0.f;
#pragma unroll
        for (int w = 0; w < 4; ++w) S[j] = fmaf(lw[w][j], __builtin_amdgcn_exp2f((mw[w][j] - M[j]) * LOG2E), S[j]);
      }
      if (lane < 4) g_store(ws + WS_GM, (lane * 256 + cu) * 16, M[0], S[0], M[1], S[1], tag);
    }
    ADVANCE(21); RD_V(t2, 21); PV(2);
    STEP_V(3); STEP_V(4); STEP_V(5); STEP_V(6);
    lds_wait(L, C_ML, 4);
    float fac;
    { const float m0 = *LDSP(float, L_ML + (lc & 7) * 8), m1 = *LDSP(float, L_ML + 64 + (lc & 7) * 8), m2 = *LDSP(float, L_ML + 128 + (lc & 7) * 8),
                  m3 = *LDSP(float, L_ML + 192 + (lc & 7) * 8), mo = *LDSP(float, L_ML + wave * 64 + (lc & 7) * 8);
      fac = __builtin_amdgcn_exp2f((mo - fmaxf(fmaxf(m0, m1), fmaxf(m2, m3))) * LOG2E); }
    STEP_V(7);
#pragma unroll
    for (int j = 0; j < 4; ++j) *LDSP(v4f, wb + (lc & 7) * 528 + (j + ((lc >> 3) << 2)) * 64 + lq * 16) = acc[j] * fac;
    lds_arrive(L, C_O, lane);
    lds_spin(L, C_O, 4);
    {
      const unsigned ro = (tid >> 5) * 528 + (tid & 31) * 16;
      const v4f o = (*LDSP(v4f, ro) + *LDSP(v4f, RING + ro)) + (*LDSP(v4f, 2 * RING + ro) + *LDSP(v4f, 3 * RING + ro));
      g_store(ws + WS_GA, (cu * 256 + tid) * 16, o[0], o[1], o[2], o[3], tag);
    }
    lds_wait(L, C_W, 4);
    float wt = *LDSP(float, L_W + tid * 4);
    asm volatile("" : "+v"(wt));
    __builtin_amdgcn_s_sleep(DLYA);
    {
      v4u a;
      asm volatile("global_load_dwordx4 %0, %1, %2 sc1\n\ts_waitcnt vmcnt(0)" : "=&v"(a) : "v"((tid * 256 + px) * 16), "s"(ws + WS_GA) : "memory");
      g_retry<0>(ws + WS_GA, (tid * 256 + px) * 16, tag, a);
      const float o = wave_sum4(ug(a.x) * wt, ug(a.y) * wt, ug(a.z) * wt, ug(a.w) * wt);
      if (lc == 0) *LDSP(u16, L_O16 + wave * 8 + lq * 2) = (u16)pk2(o, o);
    }
    BAR();
    {
      v4u t[5];
      const unsigned o = tid * 16;
      asm volatile("global_load_dwordx4 %0, %5, %10 sc1\n\tglobal_load_dwordx4 %1, %6, %10 sc1\n\tglobal_load_dwordx4 %2, %7, %10 sc1\n\t"
                   "global_load_dwordx4 %3, %8, %10 sc1\n\tglobal_load_dwordx4 %4, %9, %10 sc1\n\ts_waitcnt vmcnt(0)"
                   : "=&v"(t[0]), "=&v"(t[1]), "=&v"(t[2]), "=&v"(t[3]), "=&v"(t[4])
                   : "v"(o), "v"(o + 4096), "v"(o + 8192), "v"(o + 12288), "v"(o + 16384), "s"(ws + WS_GT) : "memory");
      bool done = false;
#pragma unroll 1
      for (int pass = 0; pass < 3; ++pass) {
        if (!done) {
          if (pass) {
#pragma unroll
            for (int k = 0; k < 5; ++k) {
              if (!g_ok(t[k], tag)) asm volatile("global_load_dwordx4 %0, %1, %2 sc1\n\ts_waitcnt vmcnt(0)" : "=&v"(t[k]) : "v"(k * 4096 + o), "s"(ws + WS_GT) : "memory");
              if (pass == 2) g_retry<4>(ws + WS_GT, k * 4096 + o, tag, t[k]);
            }
          }
          done = pass == 2 || __all(g_ok(t[0], tag) && g_ok(t[1], tag) && g_ok(t[2], tag) && g_ok(t[3], tag) && g_ok(t[4], tag));
          if (done) {
#pragma unroll
            for (int k = 0; k < 5; ++k) {
              *LDSP(unsigned, L_TAB + k * 2048 + tid * 4) = (t[k].x >> 16) | (t[k].y & 0xffff0000u);
              if (k < 4) *LDSP(unsigned, L_TAB + k * 2048 + 1024 + tid * 4) = (t[k].z >> 16) | (t[k].w & 0xffff0000u);
            }
          }
        }
        BAR();
      }
    }
  } else {
    __builtin_amdgcn_s_setprio(2);
    const int aw = wave - 4, ta = tid - 256;
    const int R0 = 256 * rb + 64 * aw, row8 = R0 + 8 * (lane >> 3);
    if (ta < 32)
      asm volatile("global_store_dwordx4 %0, %1, %2 sc1\n\ts_nop 1" ::"v"(ta < 16 ? WS_G1 + (r8 * 512 + 16 * c5 + ta) * 16 : WS_G2 + (rb * 1024 + 16 * cb + ta - 16) * 16),
                   "v"(v4u{0u, 0u, 0u, 0u}), "s"(ws) : "memory");
    __builtin_amdgcn_sched_barrier(0);
    const v4u xa = *(const v4u*)(x + 16 * lane), xb = *(const v4u*)(x + 16 * lane + 8);
    const v2u xr = *(const v2u*)(x + R0 + 4 * (lane & 15));
    const u16 xo = x[4 * px + (lane & 3)], b2r = b2[0];
    const v4u cr = *(const v4u*)(cvec + row8);
    __builtin_amdgcn_sched_barrier(0);
    const u64 e1 = (((u64)er.y << 32) | er.x) * 6364136223846793005ull + 1442695040888963407ull;
    tag = Tag{(unsigned)(e1 >> 24) & 0x3ffu, (unsigned)(e1 >> 34) & 0x3ffu, (unsigned)(e1 >> 44) & 0x3ffu, (unsigned)(e1 >> 54) & 0x3ffu};
    const Tag tagb{(((unsigned)e1 << 3) & 0xf8u) | 4u, ((unsigned)e1 >> 3) & 0xfcu, ((unsigned)e1 >> 9) & 0xfcu, ((unsigned)e1 >> 15) & 0xfcu};
    {
      float a[8] = {0, 0, 0, 0, 0, 0, 0, 0};
#pragma unroll
      for (int k = 0; k < 4; ++k) {
        const float s = silu((k & 1) ? hi16(cq[k >> 1]) : lo16(cq[k >> 1]));
#pragma unroll
        for (int j = 0; j < 4; ++j) { a[2 * j] = fmaf(s, lo16(wm[k][j]), a[2 * j]); a[2 * j + 1] = fmaf(s, hi16(wm[k][j]), a[2 * j + 1]); }
      }
      cols64(a, lane, LDSP(float, L_P2 + aw * 256));
      wait_vm<6>();
      if (lds_arrive(L, C_P2, lane) == 3 && lane < 16)
        gb_store(ws + WS_G1, (r8 * 512 + 16 * c5 + lane) * 16, (*LDSP(v4f, L_P2 + lane * 16) + *LDSP(v4f, L_P2 + 256 + lane * 16)) + (*LDSP(v4f, L_P2 + 512 + lane * 16) + *LDSP(v4f, L_P2 + 768 + lane * 16)), tagb);
      if (cu < 64 && ta == 0) g_store(ws + WS_GS, cu * 16, asf(tstart << 10), 0.f, 0.f, 0.f, tag);
    }
    float sv[8];
#pragma unroll
    for (int k = 0; k < 8; ++k) sv[k] = silu((k & 1) ? hi16(cr[k >> 1]) : lo16(cr[k >> 1]));
    v4f hn;
    float xm;
    {
      float s1 = 0.f;
#pragma unroll
      for (int j = 0; j < 4; ++j) s1 += (lo16(xa[j]) + hi16(xa[j])) + (lo16(xb[j]) + hi16(xb[j]));
      s1 = wave_sum(s1);
      const float mean = s1 * (1.f / 1024.f);
      float s2 = 0.f;
#pragma unroll
      for (int j = 0; j < 4; ++j) { float d = lo16(xa[j]) - mean; s2 = fmaf(d, d, s2); d = hi16(xa[j]) - mean; s2 = fmaf(d, d, s2);
                                    d = lo16(xb[j]) - mean; s2 = fmaf(d, d, s2); d = hi16(xb[j]) - mean; s2 = fmaf(d, d, s2); }
      s2 = wave_sum(s2);
      const float mb = rbf(mean), sd = rbf(sqrtf(rbf(rbf(s2 * (1.f / 1024.f)) + 1e-5f)));
      xm = mb;
      hn = rbf4(rbf4(v4f{lo16(xr.x), hi16(xr.x), lo16(xr.y), hi16(xr.y)} - mb) / sd);
    }
    Mix P;
    float x1own = lo16(xo);
    const float b2v = lo16(b2r);
    int tj = 0;
#define TABLE2() do { __builtin_amdgcn_sched_barrier(0); __builtin_amdgcn_s_setprio(0); \
                      const float t_ = tab_input(256 * tj + px), y_ = mixer2<true>(P, t_, -t_); \
                      if ((lane & 31) == 0) *LDSP(float, L_R3 + (2 * tj + (lane >> 5)) * 16 + aw * 4) = y_; \
                      ++tj; __builtin_amdgcn_s_setprio(2); __builtin_amdgcn_sched_barrier(0); } while (0)
#define NOW() (int)((unsigned)__builtin_amdgcn_s_memrealtime() - tstart)
    lds_wait(L, C_PP, 4);
#pragma unroll
    for (int k = 0; k < 7; ++k) {
      P.w1[k] = unp(*LDSP(unsigned, 3 * 4096 + ta * 28 + k * 4)); P.b1[k] = unp(*LDSP(unsigned, RING + 3 * 4096 + ta * 28 + k * 4));
      P.w2[k] = *LDSP(unsigned, 2 * RING + 3 * 4096 + ta * 28 + k * 4);
    }
    lds_arrive(L, C_PB, lane);
    if (NOW() < T_1 - FIT1) TABLE2();
    lds_wait(L, C_PA, 4);
    v4u wqr[8];
#pragma unroll
    for (int i = 0; i < 8; ++i) wqr[i] = *LDSP(v4u, aw * RING + 5 * 4096 + i * 1024 + lane * 16);
    lds_arrive(L, C_PD, lane);
    UNTIL(T_1);
    v4u gs0;
    {
      v4u p[4];
      const unsigned o = WS_G1 + ((lane >> 5) * 2048 + ((lane >> 4) & 1) * 256 + R0 / 4 + (lane & 15)) * 16;
#pragma unroll
      for (int k = 0; k < 4; ++k) p[k] = GLD(o + k * 8192);
      gs0 = GLD(WS_GS + lane * 16);
      TABLE2();
#pragma unroll 1
      for (int s = 0; s < SPIN_G; ++s) {
        if (__all(gb_ok(p[0], tagb) && gb_ok(p[1], tagb) && gb_ok(p[2], tagb) && gb_ok(p[3], tagb))) break;
        asm volatile("" ::: "memory");
#pragma unroll
        for (int k = 0; k < 4; ++k) if (!gb_ok(p[k], tagb)) p[k] = GLD(o + k * 8192);
      }
      const v4f m = (gb_val(p[0]) + gb_val(p[1])) + (gb_val(p[2]) + gb_val(p[3]));
#pragma unroll
      for (int k = 0; k < 4; ++k) {
        const float t = rbf(tr32(m[k], m[k]));
        const auto r = __builtin_amdgcn_permlane16_swap(asu(t), asu(t), false, false);
        hn[k] = rbf(rbf(hn[k] * rbf(1.f + asf(r[1]))) + asf(r[0]));
      }
      if (lane < 16) *LDSP(v4f, L_H + aw * 256 + lane * 16) = hn;
    }
    {
      const v4f ha = *LDSP(v4f, L_H + aw * 256 + (lane >> 3) * 32), hb = *LDSP(v4f, L_H + aw * 256 + (lane >> 3) * 32 + 16);
      const float hv[8] = {ha[0], ha[1], ha[2], ha[3], hb[0], hb[1], hb[2], hb[3]};
      gemv64(wqr, hv, lane, LDSP(float, L_P3 + aw * 256));
    }
    if (lds_arrive(L, C_P3, lane) == 3 && lane < 16)
      gb_store(ws + WS_G2, (rb * 1024 + 16 * cb + lane) * 16, (*LDSP(v4f, L_P3 + lane * 16) + *LDSP(v4f, L_P3 + 256 + lane * 16)) + (*LDSP(v4f, L_P3 + 512 + lane * 16) + *LDSP(v4f, L_P3 + 768 + lane * 16)), tagb);
    {
      g_retry(ws, WS_GS + lane * 16, tag, gs0);
      const float d = (float)min(max((int)(gs0.x - (tstart << 10)) >> 10, -300), 300);
      const int dm = (int)wave_max(d), da = (int)(wave_sum(d) * (1.f / 64.f));
      if (ta == 0) *LDSP(unsigned, L_TB) = tstart + ((da + dm) >> 1);
      if (aw == 0) lds_arrive(L, C_TB, lane);
      tstart += __builtin_amdgcn_readfirstlane(dm);
    }
    {
      lds_wait(L, C_PC, 4);
      v4u wr[8];
#pragma unroll
      for (int i = 0; i < 8; ++i) wr[i] = *LDSP(v4u, aw * RING + 7 * 4096 + i * 1024 + lane * 16);
      gemv64(wr, sv, lane, LDSP(float, L_P4 + aw * 256));
      if (lds_arrive(L, C_P4, lane) == 3 && lane < 16) {
        const v4f s = (*LDSP(v4f, L_P4 + lane * 16) + *LDSP(v4f, L_P4 + 256 + lane * 16)) + (*LDSP(v4f, L_P4 + 512 + lane * 16) + *LDSP(v4f, L_P4 + 768 + lane * 16));
        g_store(ws + WS_G3, (rb * 1024 + 16 * cc + lane) * 16, s[0], s[1], s[2], s[3], tag);
      }
    }
#pragma unroll 1
    while (tj < 6 && NOW() < T_2 - FIT) TABLE2();
    UNTIL(T_2);
    {
      v4u p[4];
      const unsigned o = WS_G2 + (g * 256 + ta) * 16;
#pragma unroll
      for (int k = 0; k < 4; ++k) p[k] = GLD(o + k * 16384);
      TABLE2();
#pragma unroll 1
      for (int s = 0; s < SPIN_G; ++s) {
        if (__all(gb_ok(p[0], tagb) && gb_ok(p[1], tagb) && gb_ok(p[2], tagb) && gb_ok(p[3], tagb))) break;
        asm volatile("" ::: "memory");
#pragma unroll
        for (int k = 0; k < 4; ++k) if (!gb_ok(p[k], tagb)) p[k] = GLD(o + k * 16384);
      }
      const v4f q = (gb_val(p[0]) + gb_val(p[1])) + (gb_val(p[2]) + gb_val(p[3]));
      *LDSP(v2u, L_Q + ta * 8) = v2u{pk2(q[0], q[1]), pk2(q[2], q[3])};
      if (ta == 0) *LDSP(v4u, L_TAG) = v4u{tag.a, tag.b, tag.c, tag.d};
      lds_arrive(L, C_Q, lane);
    }
    if (cu == 1) asm volatile("global_store_dwordx4 %0, %1, %2 sc1\n\ts_nop 1" ::"v"(ta * 16), "v"(v4u{~tag.a, ~tag.b, ~tag.c, ~tag.d}), "s"(ws + WS_GC) : "memory");
    float gaown = 0.f, gbown = 0.f;
#pragma unroll 1
    for (bool first = true; tj < 9 && (first || __builtin_amdgcn_readfirstlane(*(volatile LAS unsigned*)(LDSP(unsigned, L_CTL) + C_LAST)) == 0u); first = false) {
      const bool gates = aw == 0 && first;
      const unsigned og = (lane & 3) * 16384 + ((lane >> 2) & 3) * 4096 + px * 16;
      v4u u = {0u, 0u, 0u, 0u};
      if (gates) u = GLD(WS_G3 + og);
      TABLE2();
      if (gates) {
        g_retry(ws + WS_G3, og, tag, u);
        float v[4];
#pragma unroll
        for (int k = 0; k < 4; ++k) { v[k] = ug(u[k]); v[k] += dpp<XOR1>(v[k]); v[k] += dpp<XOR2>(v[k]); v[k] = rbf(v[k]); }
        if (lane == 4) g_store(ws + WS_GD, px * 16, v[0], v[1], v[2], v[3], tag);
        if (lane == 8) g_store(ws + WS_GD, 4096 + px * 16, rbf(1.f + v[0]), rbf(1.f + v[1]), rbf(1.f + v[2]), rbf(1.f + v[3]), tag);
        const int c = lane & 3;
        gaown = c == 0 ? v[0] : c == 1 ? v[1] : c == 2 ? v[2] : v[3];
        gbown = asf(__builtin_amdgcn_ds_bpermute((12 + c) * 4, (int)asu(gaown)));
      }
    }
#define PUBLISH_TABLE() do { if (lds_arrive(L, C_TAB, lane) == 3 && lane < 20) { \
      const v4f s = *LDSP(v4f, L_R3 + min(lane, 17) * 16); \
      const float y = rbf(rbf((s[0] + s[1]) + (s[2] + s[3])) + b2v); \
      const float y0 = dpp<0x00>(y), y1 = dpp<0x55>(y), y2 = dpp<0xAA>(y), y3 = dpp<0xFF>(y); \
      if ((lane & 3) == 0) g_store(ws + WS_GT, ((lane >> 2) * 256 + px) * 16, y0, y1, y2, y3, tag); \
    } } while (0)
    const bool early = tj == 9;
    if (early) PUBLISH_TABLE();
    lds_wait(L, C_MID, 4);
    const unsigned ogm = WS_GM + ((px >> 6) * 256 + ta) * 16;
    unsigned o0 = WS_GD;
    asm volatile("" : "+v"(o0));
    v4u gm = GLD(ogm), gs = GLD(WS_GD + ta * 16), gc = GLD(WS_GD + 4096 + ta * 16), g0 = GLD(o0);
    __builtin_amdgcn_sched_barrier(0);
    lds_wait(L, C_LAST, 4);
    v2u wor[16];
    const int tw = (int)((unsigned)__builtin_amdgcn_s_memrealtime() - tstart) + WO_0;
#pragma unroll
    for (int r = 0; r < 16; ++r) {
      if ((r & 3) == 0) { UNTIL(tw + (r >> 2) * WO_D); __builtin_amdgcn_sched_barrier(0); }
      wor[r] = ldnt<v2u>(wo + (size_t)((r >> 2) * 1024 + 4 * px + (r & 3)) * 1024 + 4 * ta);
      if ((r & 3) == 3) __builtin_amdgcn_sched_barrier(0);
    }
    v4f c1, sb;
    float sb0;
    {
      g_retry(ws, ogm, tag, gm);
      const float m = ug((px & 32) ? gm.z : gm.x), l = ug((px & 32) ? gm.w : gm.y);
      const float e = __builtin_amdgcn_exp2f((m - wave_max(m)) * LOG2E);
      *LDSP(float, L_W + ta * 4) = e / wave_sum(e * l);
      lds_arrive(L, C_W, lane);
      g_retry(ws, WS_GD + ta * 16, tag, gs); g_retry(ws, WS_GD + 4096 + ta * 16, tag, gc); g_retry(ws, o0, tag, g0);
      sb0 = ug(g0.x);
#pragma unroll
      for (int k = 0; k < 4; ++k) { sb[k] = ug(gs[k]); c1[k] = ug(gc[k]); }
    }
    if (!early) {
#pragma unroll 1
      while (tj < 9) TABLE2();
      PUBLISH_TABLE();
    }
    unsigned wp[8][4];
#define PAIR(p) do { asm volatile("" : "+v"(wor[2 * (p)]), "+v"(wor[2 * (p) + 1])); \
      wp[p][0] = __builtin_amdgcn_perm(wor[2 * (p) + 1].x, wor[2 * (p)].x, 0x05040100u); wp[p][1] = __builtin_amdgcn_perm(wor[2 * (p) + 1].x, wor[2 * (p)].x, 0x07060302u); \
      wp[p][2] = __builtin_amdgcn_perm(wor[2 * (p) + 1].y, wor[2 * (p)].y, 0x05040100u); wp[p][3] = __builtin_amdgcn_perm(wor[2 * (p) + 1].y, wor[2 * (p)].y, 0x07060302u); \
      asm volatile("" : "+v"(wp[p][0]), "+v"(wp[p][1]), "+v"(wp[p][2]), "+v"(wp[p][3])); } while (0)
    PAIR(0); PAIR(1); PAIR(2); PAIR(3); PAIR(4); PAIR(5);
    BAR();
    {
      const v4u oa = *LDSP(v4u, L_O16), ob = *LDSP(v4u, L_O16 + 16);
      float a[4] = {0.f, 0.f, 0.f, 0.f};
#pragma unroll
      for (int p = 0; p < 8; ++p) {
        if (p == 6) { asm volatile("" : "+v"(a[0]), "+v"(a[1]), "+v"(a[2]), "+v"(a[3])); PAIR(6); PAIR(7); }
#pragma unroll
        for (int c = 0; c < 4; ++c) a[c] = dot2(wp[p][c], p < 4 ? oa[p] : ob[p - 4], a[c]);
      }
      g_store(ws + WS_GB, (cu * 256 + ta) * 16, a[0], a[1], a[2], a[3], tag);
    }
    __builtin_amdgcn_s_sleep(DLYB);
    {
      const v4u a = g_wait1(ws + WS_GB, (ta * 256 + px) * 16, tag);
      const float s = wave_sum4(ug(a.x), ug(a.y), ug(a.z), ug(a.w));
      if ((lane & 15) == 0) *LDSP(float, L_RB + (lane >> 4) * 16 + aw * 4) = s;
    }
    BAR();
    if (aw == 0) {
      const v4f s = *LDSP(v4f, L_RB + (lane & 3) * 16);
      x1own = rbf(x1own + rbf(gaown * rbf((s[0] + s[1]) + (s[2] + s[3]))));
      const float a0 = dpp<0x00>(x1own), a1 = dpp<0x55>(x1own), a2 = dpp<0xAA>(x1own), a3 = dpp<0xFF>(x1own);
      if (lane == 0) g_store(ws + WS_GC, px * 16, a0, a1, a2, a3, tag);
    }
    __builtin_amdgcn_s_sleep(DLYC);
    v4f h2;
    {
      const v4u a = g_wait1(ws + WS_GC, ta * 16, tag);
      h2 = v4f{ug(a.x), ug(a.y), ug(a.z), ug(a.w)};
      const float s = moments(h2, xm);
      if ((lane & 31) == 0) *LDSP(float, L_R1 + (lane >> 5) * 16 + aw * 4) = s;
      if (cu == 0 && ta == 64) asm volatile("global_store_dwordx2 %0, %1, %2 sc1" ::"v"(0u), "v"(e1), "s"(ws + WS_E) : "memory");
    }
    BAR();
    {
      const v4f p = *LDSP(v4f, L_R1), q = *LDSP(v4f, L_R1 + 16);
      float mb, rs;
      ln_stats((p[0] + p[1]) + (p[2] + p[3]), (q[0] + q[1]) + (q[2] + q[3]), xm, mb, rs);
      h2 = rbf4(rbf4(rbf4(rbf4(h2 - mb) * rs) * c1) + sb);
      const float s = moments(h2, sb0);
      if ((lane & 31) == 0) *LDSP(float, L_R2 + (lane >> 5) * 16 + aw * 4) = s;
      if (ta == px) *LDSP(v4f, L_H2) = h2;
    }
    BAR();
    {
      const v4f p = *LDSP(v4f, L_R2), q = *LDSP(v4f, L_R2 + 16);
      const float h = *LDSP(float, L_H2 + (lane & 3) * 4);
      float mb, rs;
      ln_stats((p[0] + p[1]) + (p[2] + p[3]), (q[0] + q[1]) + (q[2] + q[3]), sb0, mb, rs);
      const float t = rbf(rbf(h - mb) * rs);
      const unsigned bits = asu(t) >> 16, idx = (bits & 0x7fffu) - TAB_LO;
      const bool zero = (bits & 0x7fffu) == 0u, in = idx < TAB_Z || zero;
      float y = lo16(*LDSP(u16, L_TAB + (in && !zero ? idx : TAB_Z) * 4 + (bits >> 15) * 2));
      if (!__all(in)) {
#pragma unroll 1
        for (int j = 0; j < 2; ++j) {
          if ((__ballot(!in) & (3ull << (2 * j))) == 0) continue;
          const float s = mixer2<false>(P, asf(__builtin_amdgcn_readlane(asu(t), 2 * j)), asf(__builtin_amdgcn_readlane(asu(t), 2 * j + 1)));
          if ((lane & 31) == 0) *LDSP(float, L_R3 + 320 + (2 * j + (lane >> 5)) * 16 + aw * 4) = s;
        }
        BAR();
        const v4f r = *LDSP(v4f, L_R3 + 320 + (lane & 3) * 16);
        if (!in) y = rbf(rbf((r[0] + r[1]) + (r[2] + r[3])) + b2v);
      }
      if (ta < 4) { const float o = x1own + rbf(gbown * y); out[4 * px + lane] = (u16)pk2(o, o); }
    }
  }
}
}

extern "C" void kernel_launch(void* const* d_in, const int* in_sizes, int n_in, void* d_out, int out_size, void* d_ws, size_t ws_size, hipStream_t stream) {
  (void)in_sizes; (void)n_in; (void)out_size; (void)ws_size;
  (void)hipFuncSetAttribute((const void*)block_kernel, hipFuncAttributeMaxDynamicSharedMemorySize, L_END);
  hipLaunchKernelGGL(block_kernel, dim3(NCU), dim3(NTHR), L_END, stream, (const u16*)d_in[0], (const unsigned char*)d_in[1], (const unsigned char*)d_in[2],
                     (const u16*)d_in[3], (const u16*)d_in[4], (const u16*)d_in[6], (const u16*)d_in[7], (const u16*)d_in[8], (const u16*)d_in[9],
                     (const u16*)d_in[10], (const u16*)d_in[11], (u16*)d_out, (unsigned char*)d_ws);
}
```
